```python
import torch

def _softmax(x, axis=-1):
    return torch.softmax(x, dim=axis)
def _gelu(x):
    return torch.nn.functional.gelu(x, approximate="tanh")
def _silu(x):
    return torch.nn.functional.silu(x)
def _sigmoid(x):
    return torch.sigmoid(x)
def _layernorm(x, axis=-1, eps=1e-5):
    m = x.mean(dim=axis, keepdim=True)
    v = x.var(dim=axis, keepdim=True, unbiased=False)
    return (x - m) / torch.sqrt(v + eps)
def _scatter_add(tgt, idx, src, axis=0):
    return tgt.clone().index_add_(axis, idx, src)
def _segment_sum(src, seg, n):
    out = torch.zeros((n,) + src.shape[1:], dtype=src.dtype)
    return out.index_add_(0, seg, src)
def _pad(x, pw):
    flat = tuple(v for pair in reversed(pw) for v in pair)
    return torch.nn.functional.pad(x, flat)

kernel_name = 'concat_heads__sliding_block__softmax_full_mha__prompt'

def setup_inputs():

    _w = (lambda a: (lambda t: t.to(torch.bfloat16) if t.is_floating_point() else t)(torch.as_tensor(a)))
    def _A_setup():
        B, S, D = 1, 2048, 640
        return {
            "x": _w((lambda *s: torch.randn(*s, dtype=torch.bfloat16) * 0.4)(B, S, D)),
            "wqkv": _w((lambda *s: torch.randn(*s, dtype=torch.bfloat16) * 0.4)(D, 3 * D)),
            "wo": _w((lambda *s: torch.randn(*s, dtype=torch.bfloat16) * 0.05)(D, D)),
        }
    _A_in = _A_setup()

    def _B_setup():
        B, S, D = 1, 2048, 640
        return {
            "x": _w((lambda *s: torch.randn(*s, dtype=torch.bfloat16) * 0.2)(B, S, D)),
            "wqkv": _w((lambda *s: torch.randn(*s, dtype=torch.bfloat16) * 0.4)(D, 3 * D)),
            "wo": _w((lambda *s: torch.randn(*s, dtype=torch.bfloat16) * 0.1)(D, D)),
        }
    _B_in = _B_setup()

    return {
        "A_x": _A_in["x"],
        "A_wqkv": _A_in["wqkv"],
        "A_wo": _A_in["wo"],
        "B_x": _B_in["x"],
        "B_wqkv": _B_in["wqkv"],
        "B_wo": _B_in["wo"],
    }

def reference(A_x, A_wqkv, A_wo, B_x, B_wqkv, B_wo):
    def _A_ref(x, wqkv, wo):
        B, S, D = x.shape
        H = 32; Dh = D // H; W = 256
        qkv = (lambda x, *d: x.permute(*d))((x @ wqkv).reshape(B, S, 3, H, Dh), 2, 0, 3, 1, 4)
        q, k, v = qkv[0], qkv[1], qkv[2]
        scores = torch.einsum("bhqd,bhkd->bhqk", q, k) * (Dh ** -0.5)
        idx = torch.arange(S)
        mask = (idx[:, None] - idx[None, :] >= 0) & (idx[:, None] - idx[None, :] < W)
        scores = torch.where(mask[None, None], scores, -1e9)
        p = _softmax(scores, axis=-1)
        o = torch.einsum("bhqk,bhkd->bhqd", p, v)
        return (lambda x, *d: x.permute(*d))(o, 0, 2, 1, 3).reshape(B, S, D) @ wo

    def _B_ref(x, wqkv, wo):
        B, S, D = x.shape
        H = 32; Dh = D // H
        qkv = (lambda x, *d: x.permute(*d))((x @ wqkv).reshape(B, S, 3, H, Dh), 2, 0, 3, 1, 4)
        q, k, v = qkv[0], qkv[1], qkv[2]
        scale = Dh ** -0.5
        scores = torch.einsum("bhqd,bhkd->bhqk", q, k) * scale
        p = _softmax(scores, axis=-1)
        o = torch.einsum("bhqk,bhkd->bhqd", p, v)
        return (lambda x, *d: x.permute(*d))(o, 0, 2, 1, 3).reshape(B, S, D) @ wo

    a = _A_ref(A_x, A_wqkv, A_wo)
    b = _B_ref(B_x, B_wqkv, B_wo)
    return torch.concatenate([a, b], axis=-1)
```

```cpp
#include <hip/hip_runtime.h>
#include <hip/hip_bf16.h>
#include <stdint.h>

typedef __bf16 bf16x8 __attribute__((ext_vector_type(8)));
typedef short s16x4 __attribute__((ext_vector_type(4)));
typedef float f32x16 __attribute__((ext_vector_type(16)));
typedef float f32x4 __attribute__((ext_vector_type(4)));
typedef float f32x2 __attribute__((ext_vector_type(2)));
typedef __bf16 bf16x2 __attribute__((ext_vector_type(2)));
typedef unsigned u32x4 __attribute__((ext_vector_type(4)));
typedef unsigned u32x2 __attribute__((ext_vector_type(2)));
typedef unsigned long long u64;
typedef __amdgpu_buffer_rsrc_t rsrc_t;
#define LDSP __attribute__((address_space(3)))
#define GAS __attribute__((address_space(1)))
#define DI __device__ __forceinline__

constexpr int SEQ = 2048, DM = 640, N3 = 1920, NH = 32;
constexpr int QT = 1280;
constexpr int KVT = 2640;
constexpr unsigned QF_BYTES = NH * 64 * QT, KV_BYTES = NH * 64 * KVT;
constexpr size_t WS_QF = 0, WS_KV = 2 * (size_t)QF_BYTES, WS_OF = WS_KV + 2 * (size_t)KV_BYTES, WS_SYNC = 32u << 20;
constexpr unsigned SY_GB = 4096, SY_GA = SY_GB + 256 * 64, SY_TB = SY_GA + 256 * 64, SY_TA = SY_TB + 256 * 64, SY_BYTES = SY_TA + 256 * 64;
constexpr float QSCALE = 0.32259784f;
constexpr unsigned OOB = 0x40000000u;
constexpr int SC1 = 16;
constexpr int LDS_AIMG = 0, LDS_PART = 16 * 3584, LDS_WO = 16 * 3584, LDS_BYTES = 163840;

DI unsigned cvtpk(float lo, float hi) { f32x2 v = {lo, hi}; bf16x2 b = __builtin_convertvector(v, bf16x2); return __builtin_bit_cast(unsigned, b); }
DI unsigned rfl(unsigned x) { return __builtin_amdgcn_readfirstlane(x); }
DI u32x4 make_srd(const void* p, unsigned bytes) {
    const uint64_t a = (uint64_t)p;
    u32x4 r = {rfl((unsigned)a), rfl((unsigned)(a >> 32) & 0xffffu), rfl(bytes), 0x00020000u};
    return r;
}
DI void dma16(unsigned lds_base, unsigned voff, u32x4 srd, unsigned soff) {
    asm volatile("s_mov_b32 m0, %0\n\ts_nop 0\n\tbuffer_load_dwordx4 %1, %2, %3 offen lds" :: "s"(lds_base), "v"(voff), "s"(srd), "s"(soff) : "memory");
}
DI rsrc_t mk_rsrc(const void* p, unsigned bytes) { return __builtin_amdgcn_make_buffer_rsrc((void*)p, 0, bytes, 0x00020000); }
DI void st16(rsrc_t r, unsigned off, u32x4 v) { __builtin_amdgcn_raw_buffer_store_b128(v, r, off, 0, SC1); }
DI void st8(rsrc_t r, unsigned off, u32x2 v) { __builtin_amdgcn_raw_buffer_store_b64(v, r, off, 0, SC1); }
DI u32x4 ld16(rsrc_t r, unsigned off) { return __builtin_amdgcn_raw_buffer_load_b128(r, off, 0, SC1); }
DI u32x2 ld8(rsrc_t r, unsigned off) { return __builtin_amdgcn_raw_buffer_load_b64(r, off, 0, SC1); }
DI s16x4 lds_tr(LDSP const char* p) { return __builtin_amdgcn_ds_read_tr16_b64_v4i16((LDSP s16x4*)p); }
DI bf16x8 cat(s16x4 a, s16x4 b) { return __builtin_bit_cast(bf16x8, __builtin_shufflevector(a, b, 0, 1, 2, 3, 4, 5, 6, 7)); }
#define MFMA32(a, b, c) __builtin_amdgcn_mfma_f32_32x32x16_bf16((a), (b), (c), 0, 0, 0)
#define MFMA32K8(a, b, c) __builtin_amdgcn_mfma_f32_32x32x8bf16_1k((a), (b), (c), 0, 0, 0)
#define MFMA16(a, b, c) __builtin_amdgcn_mfma_f32_16x16x32_bf16((a), (b), (c), 0, 0, 0)
#define BARRIER_DRAINED() asm volatile("s_waitcnt vmcnt(0) lgkmcnt(0)\n\ts_barrier" ::: "memory")

#define PF_XW 0
#define STAMP(i) do {} while (0)

DI void publish(char* ws, unsigned off, u64 tag) { if ((threadIdx.x & 63) == 0) __hip_atomic_store((GAS u64*)(ws + WS_SYNC + off), tag, __ATOMIC_RELAXED, __HIP_MEMORY_SCOPE_AGENT); }
template <int NF> DI void wait_flags(const char* ws, unsigned first, unsigned stride, u64 tag) {
    const rsrc_t rf = mk_rsrc(ws + WS_SYNC, SY_BYTES);
    const unsigned off = first + (threadIdx.x & 63) * stride;
    for (int spin = 0; spin < (1 << 16); ++spin) {
        u32x2 v[NF];
#pragma unroll
        for (int k = 0; k < NF; ++k) v[k] = ld8(rf, off + k * 64 * stride);
        bool ok = true;
#pragma unroll
        for (int k = 0; k < NF; ++k) ok = ok & (v[k][0] == (unsigned)tag) & (v[k][1] == (unsigned)(tag >> 32));
        if (__all(ok)) break;
        __builtin_amdgcn_s_sleep(2);
        asm volatile("" ::: "memory");
    }
    asm volatile("" ::: "memory");
}

template <int R>
DI void g_epilogue_qk(rsrc_t rb, unsigned sbase, unsigned tstride, unsigned v16, unsigned v8, float sc, const f32x16& acc00, const f32x16& acc01, const f32x16& acc10, const f32x16& acc11) {
    const unsigned hstride = 64 * tstride;
#pragma unroll
    for (int idx = 0; idx < 8; ++idx) {
        const int ni = idx >> 2, g = idx & 3, p5 = (R + idx) % 5, hp = (R + idx) / 5;
        const f32x16& a0 = ni == 0 ? acc00 : acc01; const f32x16& a1 = ni == 0 ? acc10 : acc11;
        const unsigned x0 = cvtpk(a0[4 * g] * sc, a0[4 * g + 1] * sc), x1 = cvtpk(a0[4 * g + 2] * sc, a0[4 * g + 3] * sc);
        const unsigned y0 = cvtpk(a1[4 * g] * sc, a1[4 * g + 1] * sc), y1 = cvtpk(a1[4 * g + 2] * sc, a1[4 * g + 3] * sc);
        if (p5 == 2) {
            const u32x2 vx_ = {x0, x1}, vy_ = {y0, y1};
            const unsigned so = sbase + 2 * hp * hstride;
            st8(rb, v8 + so, vx_); st8(rb, v8 + so + tstride, vy_);
        } else {
            const auto s0 = __builtin_amdgcn_permlane32_swap(x0, y0, false, false), s1 = __builtin_amdgcn_permlane32_swap(x1, y1, false, false);
            const u32x4 v = {s0[0], s1[0], s0[1], s1[1]};
            st16(rb, v16 + (sbase + (2 * hp + (p5 > 2)) * hstride + (p5 > 2 ? p5 - 3 : p5) * 512), v);
        }
    }
}

DI void g_touch(u32x4 sx, u32x4 sw, int part, int kt, unsigned ldsb) {
    const int lane = threadIdx.x & 63, xcd = blockIdx.x & 7, wi = blockIdx.x >> 3;
    const unsigned vo = part == 0 ? ((xcd >> 1) * 512 + wi * 16 + lane) * (DM * 2) : (wi * 2 + part - 1) * (N3 * 2) + (xcd & 1) * 2048 + lane * 128;
    asm volatile("s_mov_b32 m0, %0\n\ts_nop 0\n\tbuffer_load_dword %1, %2, %3 offen lds"
                 :: "s"(ldsb + 131072), "v"(lane < 16 && part < 3 ? vo : OOB), "s"(part == 0 ? sx : sw), "s"(part == 0 ? kt * 128 : kt * 64 * (N3 * 2)) : "memory");
}

DI void g_consumer(const char* xA, const char* wA, const char* xB, const char* wB, int mt, int nt, unsigned ldsb, char* ws) {
    const int lane = threadIdx.x & 63, wid = rfl(threadIdx.x >> 6), l31 = lane & 31, h2 = lane >> 5;
    const int ws_ = wid & 1, wn = wid >> 1;
    unsigned xb[4], wb[2][2];
    { const int row = ws_ * 64 + l31, m = (row >> 1) & 7;
#pragma unroll
      for (int kk = 0; kk < 4; ++kk) xb[kk] = ldsb + row * 128 + ((16 * (h2 ^ (m & 1)) + 32 * ((m >> 1))) ^ (32 * kk)); }
    { const int blk = (lane >> 4) & 1, q = (lane & 15) >> 2, p = lane & 3;
#pragma unroll
      for (int ni = 0; ni < 2; ++ni)
#pragma unroll
        for (int t = 0; t < 2; ++t) { const int row = 8 * h2 + 4 * t + q, ch = 4 * (2 * wn + ni) + 2 * blk + (p >> 1), mask = (q << 2) | ((2 * h2 + t) & 3);
            wb[ni][t] = ldsb + 16384 + 256 * row + 16 * (ch ^ mask) + 8 * (p & 1); } }
    const bool vpart = nt >= 10, isq = nt < 5;
    const int tile0 = mt * 4 + ws_ * 2;
    unsigned e0, e1;
    if (vpart) {
        const int n0 = nt * 128 + wn * 64 + l31 - 1280, h0 = n0 / 20, n1 = n0 + 32, h1 = n1 / 20;
        e0 = (h0 * 64 + tile0) * KVT + 1296 + (h2 * 21 + n0 - h0 * 20) * 16; e1 = (h1 * 64 + tile0) * KVT + 1296 + (h2 * 21 + n1 - h1 * 20) * 16;
    } else {
        const unsigned ts = isq ? QT : KVT;
        e0 = h2 * ts + l31 * 16; e1 = h2 * 64 * ts + 1024 + l31 * 8;
    }
    asm volatile("" : "+v"(e0), "+v"(e1));
    const int pi0 = (nt * 128 + wn * 64 - (isq ? 0 : 640)) >> 3, hp0 = pi0 / 5, r5 = pi0 - hp0 * 5;
#define G_OPERANDS \
            : [acc00] "+v"(acc00), [acc01] "+v"(acc01), [acc10] "+v"(acc10), [acc11] "+v"(acc11) \
            : [xb0] "v"(xb[0]), [xb1] "v"(xb[1]), [xb2] "v"(xb[2]), [xb3] "v"(xb[3]), \
              [wb00] "v"(wb[0][0]), [wb01] "v"(wb[0][1]), [wb10] "v"(wb[1][0]), [wb11] "v"(wb[1][1]), [s0] "s"(s0) \
            : "v128", "v129", "v130", "v131", "v132", "v133", "v134", "v135", "v136", "v137", "v138", "v139", "v140", "v141", "v142", "v143", "v144", "v145", "v146", "v147", "v148", "v149", "v150", "v151", "v152", "v153", "v154", "v155", "v156", "v157", "v158", "v159", "v160", "v161", "v162", "v163", "v164", "v165", "v166", "v167", "v168", "v169", "v170", "v171", "v172", "v173", "v174", "v175", "v176", "v177", "v178", "v179", "v180", "v181", "v182", "v183", "v184", "v185", "v186", "v187", "v188", "v189", "v190", "v191", "v192", "v193", "v194", "v195", "v196", "v197", "v198", "v199", "s60", "s61", "scc", "memory"
#pragma unroll 1
    for (int pass = 0; pass < 2; ++pass) {
        const unsigned s0 = rfl(pass * 65536);
        f32x16 acc00 = {}, acc01 = {}, acc10 = {}, acc11 = {};
        if (pass == 1) STAMP(11);
        if (vpart) {
            asm volatile(
        "s_nop 4\n\t"
        "s_mov_b32 s61, %[s0]\n\t"
        "v_add_u32 v192, s61, %[xb0]\n\t"
        "v_add_u32 v193, s61, %[xb1]\n\t"
        "v_add_u32 v194, s61, %[xb2]\n\t"
        "v_add_u32 v195, s61, %[xb3]\n\t"
        "v_add_u32 v196, s61, %[wb00]\n\t"
        "v_add_u32 v197, s61, %[wb01]\n\t"
        "v_add_u32 v198, s61, %[wb10]\n\t"
        "v_add_u32 v199, s61, %[wb11]\n\t"
        "s_add_u32 s61, s61, 0x8000\n\t"
        "s_and_b32 s61, s61, 0x1ffff\n\t"
        "s_cmp_eq_u32 %[s0], 0\n\t"
        "s_cbranch_scc0 LG0_%=\n\t"
        "s_barrier\n\t"
        "LG0_%=:\n\t"
        "ds_read_b128 v[128:131], v192 offset:0\n\t"
        "ds_read_b64_tr_b16 v[136:137], v196 offset:0\n\t"
        "ds_read_b64_tr_b16 v[138:139], v197 offset:0\n\t"
        "ds_read_b128 v[132:135], v192 offset:4096\n\t"
        "ds_read_b64_tr_b16 v[140:141], v198 offset:0\n\t"
        "ds_read_b64_tr_b16 v[142:143], v199 offset:0\n\t"
        "ds_read_b128 v[144:147], v193 offset:0\n\t"
        "ds_read_b64_tr_b16 v[152:153], v196 offset:4096\n\t"
        "ds_read_b64_tr_b16 v[154:155], v197 offset:4096\n\t"
        "ds_read_b128 v[148:151], v193 offset:4096\n\t"
        "ds_read_b64_tr_b16 v[156:157], v198 offset:4096\n\t"
        "ds_read_b64_tr_b16 v[158:159], v199 offset:4096\n\t"
        "s_mov_b32 s60, 10\n\t"
        "LG_%=:\n\t"
        "s_waitcnt lgkmcnt(9)\n\t"
        "v_mfma_f32_32x32x16_bf16 %[acc00], v[128:131], v[136:139], %[acc00]\n\t"
        "ds_read_b128 v[160:163], v194 offset:0\n\t"
        "ds_read_b64_tr_b16 v[168:169], v196 offset:8192\n\t"
        "s_waitcnt lgkmcnt(10)\n\t"
        "v_mfma_f32_32x32x16_bf16 %[acc10], v[132:135], v[136:139], %[acc10]\n\t"
        "ds_read_b64_tr_b16 v[170:171], v197 offset:8192\n\t"
        "ds_read_b128 v[164:167], v194 offset:4096\n\t"
        "s_waitcnt lgkmcnt(10)\n\t"
        "v_mfma_f32_32x32x16_bf16 %[acc01], v[128:131], v[140:143], %[acc01]\n\t"
        "ds_read_b64_tr_b16 v[172:173], v198 offset:8192\n\t"
        "ds_read_b64_tr_b16 v[174:175], v199 offset:8192\n\t"
        "v_mfma_f32_32x32x16_bf16 %[acc11], v[132:135], v[140:143], %[acc11]\n\t"
        "s_waitcnt lgkmcnt(9)\n\t"
        "v_mfma_f32_32x32x16_bf16 %[acc00], v[144:147], v[152:155], %[acc00]\n\t"
        "ds_read_b128 v[176:179], v195 offset:0\n\t"
        "ds_read_b64_tr_b16 v[184:185], v196 offset:12288\n\t"
        "s_waitcnt lgkmcnt(10)\n\t"
        "v_mfma_f32_32x32x16_bf16 %[acc10], v[148:151], v[152:155], %[acc10]\n\t"
        "ds_read_b64_tr_b16 v[186:187], v197 offset:12288\n\t"
        "ds_read_b128 v[180:183], v195 offset:4096\n\t"
        "s_waitcnt lgkmcnt(10)\n\t"
        "v_mfma_f32_32x32x16_bf16 %[acc01], v[144:147], v[156:159], %[acc01]\n\t"
        "ds_read_b64_tr_b16 v[188:189], v198 offset:12288\n\t"
        "ds_read_b64_tr_b16 v[190:191], v199 offset:12288\n\t"
        "v_mfma_f32_32x32x16_bf16 %[acc11], v[148:151], v[156:159], %[acc11]\n\t"
        "s_waitcnt lgkmcnt(9)\n\t"
        "v_mfma_f32_32x32x16_bf16 %[acc00], v[160:163], v[168:171], %[acc00]\n\t"
        "s_waitcnt lgkmcnt(8)\n\t"
        "v_mfma_f32_32x32x16_bf16 %[acc10], v[164:167], v[168:171], %[acc10]\n\t"
        "s_cmp_eq_u32 s60, 7\n\t"
        "s_cbranch_scc0 LG1_%=\n\t"
        "s_waitcnt vmcnt(0)\n\t"
        "LG1_%=:\n\t"
        "s_waitcnt lgkmcnt(0)\n\t"
        "s_barrier\n\t"
        "v_add_u32 v192, s61, %[xb0]\n\t"
        "v_add_u32 v193, s61, %[xb1]\n\t"
        "v_add_u32 v194, s61, %[xb2]\n\t"
        "v_add_u32 v195, s61, %[xb3]\n\t"
        "v_add_u32 v196, s61, %[wb00]\n\t"
        "v_add_u32 v197, s61, %[wb01]\n\t"
        "v_add_u32 v198, s61, %[wb10]\n\t"
        "v_add_u32 v199, s61, %[wb11]\n\t"
        "s_add_u32 s61, s61, 0x8000\n\t"
        "s_and_b32 s61, s61, 0x1ffff\n\t"
        "ds_read_b128 v[128:131], v192 offset:0\n\t"
        "ds_read_b64_tr_b16 v[136:137], v196 offset:0\n\t"
        "ds_read_b64_tr_b16 v[138:139], v197 offset:0\n\t"
        "v_mfma_f32_32x32x16_bf16 %[acc01], v[160:163], v[172:175], %[acc01]\n\t"
        "ds_read_b128 v[132:135], v192 offset:4096\n\t"
        "ds_read_b64_tr_b16 v[140:141], v198 offset:0\n\t"
        "ds_read_b64_tr_b16 v[142:143], v199 offset:0\n\t"
        "v_mfma_f32_32x32x16_bf16 %[acc11], v[164:167], v[172:175], %[acc11]\n\t"
        "v_mfma_f32_32x32x16_bf16 %[acc00], v[176:179], v[184:187], %[acc00]\n\t"
        "ds_read_b128 v[144:147], v193 offset:0\n\t"
        "ds_read_b64_tr_b16 v[152:153], v196 offset:4096\n\t"
        "v_mfma_f32_32x32x16_bf16 %[acc10], v[180:183], v[184:187], %[acc10]\n\t"
        "ds_read_b64_tr_b16 v[154:155], v197 offset:4096\n\t"
        "ds_read_b128 v[148:151], v193 offset:4096\n\t"
        "v_mfma_f32_32x32x16_bf16 %[acc01], v[176:179], v[188:191], %[acc01]\n\t"
        "ds_read_b64_tr_b16 v[156:157], v198 offset:4096\n\t"
        "ds_read_b64_tr_b16 v[158:159], v199 offset:4096\n\t"
        "v_mfma_f32_32x32x16_bf16 %[acc11], v[180:183], v[188:191], %[acc11]\n\t"
        "s_sub_u32 s60, s60, 1\n\t"
        "s_cmp_lg_u32 s60, 0\n\t"
        "s_cbranch_scc1 LG_%=\n\t"
        "s_waitcnt lgkmcnt(0)\n\t"
        "s_nop 15\n\t"
                G_OPERANDS);
        } else {
            asm volatile(
        "s_nop 4\n\t"
        "s_mov_b32 s61, %[s0]\n\t"
        "v_add_u32 v192, s61, %[xb0]\n\t"
        "v_add_u32 v193, s61, %[xb1]\n\t"
        "v_add_u32 v194, s61, %[xb2]\n\t"
        "v_add_u32 v195, s61, %[xb3]\n\t"
        "v_add_u32 v196, s61, %[wb00]\n\t"
        "v_add_u32 v197, s61, %[wb01]\n\t"
        "v_add_u32 v198, s61, %[wb10]\n\t"
        "v_add_u32 v199, s61, %[wb11]\n\t"
        "s_add_u32 s61, s61, 0x8000\n\t"
        "s_and_b32 s61, s61, 0x1ffff\n\t"
        "s_cmp_eq_u32 %[s0], 0\n\t"
        "s_cbranch_scc0 LG0_%=\n\t"
        "s_barrier\n\t"
        "LG0_%=:\n\t"
        "ds_read_b128 v[128:131], v192 offset:0\n\t"
        "ds_read_b64_tr_b16 v[136:137], v196 offset:0\n\t"
        "ds_read_b64_tr_b16 v[138:139], v197 offset:0\n\t"
        "ds_read_b128 v[132:135], v192 offset:4096\n\t"
        "ds_read_b64_tr_b16 v[140:141], v198 offset:0\n\t"
        "ds_read_b64_tr_b16 v[142:143], v199 offset:0\n\t"
        "ds_read_b128 v[144:147], v193 offset:0\n\t"
        "ds_read_b64_tr_b16 v[152:153], v196 offset:4096\n\t"
        "ds_read_b64_tr_b16 v[154:155], v197 offset:4096\n\t"
        "ds_read_b128 v[148:151], v193 offset:4096\n\t"
        "ds_read_b64_tr_b16 v[156:157], v198 offset:4096\n\t"
        "ds_read_b64_tr_b16 v[158:159], v199 offset:4096\n\t"
        "s_mov_b32 s60, 10\n\t"
        "LG_%=:\n\t"
        "s_waitcnt lgkmcnt(9)\n\t"
        "v_mfma_f32_32x32x16_bf16 %[acc00], v[136:139], v[128:131], %[acc00]\n\t"
        "ds_read_b128 v[160:163], v194 offset:0\n\t"
        "ds_read_b64_tr_b16 v[168:169], v196 offset:8192\n\t"
        "s_waitcnt lgkmcnt(10)\n\t"
        "v_mfma_f32_32x32x16_bf16 %[acc10], v[136:139], v[132:135], %[acc10]\n\t"
        "ds_read_b64_tr_b16 v[170:171], v197 offset:8192\n\t"
        "ds_read_b128 v[164:167], v194 offset:4096\n\t"
        "s_waitcnt lgkmcnt(10)\n\t"
        "v_mfma_f32_32x32x16_bf16 %[acc01], v[140:143], v[128:131], %[acc01]\n\t"
        "ds_read_b64_tr_b16 v[172:173], v198 offset:8192\n\t"
        "ds_read_b64_tr_b16 v[174:175], v199 offset:8192\n\t"
        "v_mfma_f32_32x32x16_bf16 %[acc11], v[140:143], v[132:135], %[acc11]\n\t"
        "s_waitcnt lgkmcnt(9)\n\t"
        "v_mfma_f32_32x32x16_bf16 %[acc00], v[152:155], v[144:147], %[acc00]\n\t"
        "ds_read_b128 v[176:179], v195 offset:0\n\t"
        "ds_read_b64_tr_b16 v[184:185], v196 offset:12288\n\t"
        "s_waitcnt lgkmcnt(10)\n\t"
        "v_mfma_f32_32x32x16_bf16 %[acc10], v[152:155], v[148:151], %[acc10]\n\t"
        "ds_read_b64_tr_b16 v[186:187], v197 offset:12288\n\t"
        "ds_read_b128 v[180:183], v195 offset:4096\n\t"
        "s_waitcnt lgkmcnt(10)\n\t"
        "v_mfma_f32_32x32x16_bf16 %[acc01], v[156:159], v[144:147], %[acc01]\n\t"
        "ds_read_b64_tr_b16 v[188:189], v198 offset:12288\n\t"
        "ds_read_b64_tr_b16 v[190:191], v199 offset:12288\n\t"
        "v_mfma_f32_32x32x16_bf16 %[acc11], v[156:159], v[148:151], %[acc11]\n\t"
        "s_waitcnt lgkmcnt(9)\n\t"
        "v_mfma_f32_32x32x16_bf16 %[acc00], v[168:171], v[160:163], %[acc00]\n\t"
        "s_waitcnt lgkmcnt(8)\n\t"
        "v_mfma_f32_32x32x16_bf16 %[acc10], v[168:171], v[164:167], %[acc10]\n\t"
        "s_cmp_eq_u32 s60, 7\n\t"
        "s_cbranch_scc0 LG1_%=\n\t"
        "s_waitcnt vmcnt(0)\n\t"
        "LG1_%=:\n\t"
        "s_waitcnt lgkmcnt(0)\n\t"
        "s_barrier\n\t"
        "v_add_u32 v192, s61, %[xb0]\n\t"
        "v_add_u32 v193, s61, %[xb1]\n\t"
        "v_add_u32 v194, s61, %[xb2]\n\t"
        "v_add_u32 v195, s61, %[xb3]\n\t"
        "v_add_u32 v196, s61, %[wb00]\n\t"
        "v_add_u32 v197, s61, %[wb01]\n\t"
        "v_add_u32 v198, s61, %[wb10]\n\t"
        "v_add_u32 v199, s61, %[wb11]\n\t"
        "s_add_u32 s61, s61, 0x8000\n\t"
        "s_and_b32 s61, s61, 0x1ffff\n\t"
        "ds_read_b128 v[128:131], v192 offset:0\n\t"
        "ds_read_b64_tr_b16 v[136:137], v196 offset:0\n\t"
        "ds_read_b64_tr_b16 v[138:139], v197 offset:0\n\t"
        "v_mfma_f32_32x32x16_bf16 %[acc01], v[172:175], v[160:163], %[acc01]\n\t"
        "ds_read_b128 v[132:135], v192 offset:4096\n\t"
        "ds_read_b64_tr_b16 v[140:141], v198 offset:0\n\t"
        "ds_read_b64_tr_b16 v[142:143], v199 offset:0\n\t"
        "v_mfma_f32_32x32x16_bf16 %[acc11], v[172:175], v[164:167], %[acc11]\n\t"
        "v_mfma_f32_32x32x16_bf16 %[acc00], v[184:187], v[176:179], %[acc00]\n\t"
        "ds_read_b128 v[144:147], v193 offset:0\n\t"
        "ds_read_b64_tr_b16 v[152:153], v196 offset:4096\n\t"
        "v_mfma_f32_32x32x16_bf16 %[acc10], v[184:187], v[180:183], %[acc10]\n\t"
        "ds_read_b64_tr_b16 v[154:155], v197 offset:4096\n\t"
        "ds_read_b128 v[148:151], v193 offset:4096\n\t"
        "v_mfma_f32_32x32x16_bf16 %[acc01], v[188:191], v[176:179], %[acc01]\n\t"
        "ds_read_b64_tr_b16 v[156:157], v198 offset:4096\n\t"
        "ds_read_b64_tr_b16 v[158:159], v199 offset:4096\n\t"
        "v_mfma_f32_32x32x16_bf16 %[acc11], v[188:191], v[180:183], %[acc11]\n\t"
        "s_sub_u32 s60, s60, 1\n\t"
        "s_cmp_lg_u32 s60, 0\n\t"
        "s_cbranch_scc1 LG_%=\n\t"
        "s_waitcnt lgkmcnt(0)\n\t"
        "s_nop 15\n\t"
                G_OPERANDS);
        }
        STAMP(1 + pass);
        char* qf = ws + WS_QF + (pass == 0 ? QF_BYTES : 0); char* kv = ws + WS_KV + (pass == 0 ? KV_BYTES : 0);
        if (vpart) {
            const rsrc_t rk = mk_rsrc(kv, KV_BYTES);
#pragma unroll
            for (int ni = 0; ni < 2; ++ni)
#pragma unroll
                for (int si = 0; si < 2; ++si) {
                    const f32x16& a = si == 0 ? (ni == 0 ? acc00 : acc01) : (ni == 0 ? acc10 : acc11);
#pragma unroll
                    for (int f = 0; f < 2; ++f) {
                        const u32x4 v = {cvtpk(a[8 * f], a[8 * f + 1]), cvtpk(a[8 * f + 2], a[8 * f + 3]), cvtpk(a[8 * f + 4], a[8 * f + 5]), cvtpk(a[8 * f + 6], a[8 * f + 7])};
                        st16(rk, (ni == 0 ? e0 : e1) + si * KVT + f * 672, v);
                    }
                }
        } else {
            const unsigned ts = isq ? QT : KVT;
            const rsrc_t rb = mk_rsrc(isq ? qf : kv, isq ? QF_BYTES : KV_BYTES);
            const unsigned sbase = rfl((2 * hp0 * 64 + tile0) * ts); const float sc = isq ? QSCALE : 1.0f;
            switch (r5) {
                case 0: g_epilogue_qk<0>(rb, sbase, ts, e0, e1, sc, acc00, acc01, acc10, acc11); break;
                case 1: g_epilogue_qk<1>(rb, sbase, ts, e0, e1, sc, acc00, acc01, acc10, acc11); break;
                case 2: g_epilogue_qk<2>(rb, sbase, ts, e0, e1, sc, acc00, acc01, acc10, acc11); break;
                case 3: g_epilogue_qk<3>(rb, sbase, ts, e0, e1, sc, acc00, acc01, acc10, acc11); break;
                default: g_epilogue_qk<4>(rb, sbase, ts, e0, e1, sc, acc00, acc01, acc10, acc11); break;
            }
        }
    }
}

DI u64 read_tag(LDSP const char* p) { const u64 t = *(volatile LDSP const u64*)p; return ((u64)rfl((unsigned)(t >> 32)) << 32) | rfl((unsigned)t); }

DI void g_loader(const char* xA, const char* wA, const char* xB, const char* wB, int mt, int nt, unsigned ldsb, char* ws, LDSP const char* tagw, u64& tag) {
    const int lane = threadIdx.x & 63, lw = rfl(threadIdx.x >> 6) - 4;
    const u32x4 sxA = make_srd(xA, SEQ * DM * 2), swA = make_srd(wA, DM * N3 * 2), sxB = make_srd(xB, SEQ * DM * 2), swB = make_srd(wB, DM * N3 * 2);
    unsigned vx[4], vw[4];
#pragma unroll
    for (int j = 0; j < 4; ++j) { const int jj = lw * 4 + j, row = 8 * jj + (lane >> 3), c = (lane & 7) ^ ((row >> 1) & 7); vx[j] = (mt * 128 + row) * (DM * 2) + c * 16; }
#pragma unroll
    for (int j = 0; j < 4; ++j) { const int jj = lw * 4 + j, row = 4 * jj + (lane >> 4), ch = (lane & 15) ^ (((row & 3) << 2) | ((row >> 2) & 3)); vw[j] = row * (N3 * 2) + nt * 256 + ch * 16; }
#pragma unroll
    for (int j = 0; j < 4; ++j) { vx[j] -= j * 1024; vw[j] -= j * 1024; }
    auto issue = [&](int s) {
        const bool a = s >= 10; const int kt = a ? s - 10 : s;
        const u32x4 sx = a ? sxA : sxB, sw = a ? swA : swB;
        const unsigned base = ldsb + (s & 3) * 32768 + lw * 4096;
        asm volatile("s_mov_b32 m0, %0\n\ts_nop 0\n\t"
                     "buffer_load_dwordx4 %2, %10, %12 offen lds\n\tbuffer_load_dwordx4 %3, %10, %12 offen offset:1024 lds\n\t"
                     "buffer_load_dwordx4 %4, %10, %12 offen offset:2048 lds\n\tbuffer_load_dwordx4 %5, %10, %12 offen offset:3072 lds\n\t"
                     "s_mov_b32 m0, %1\n\ts_nop 0\n\t"
                     "buffer_load_dwordx4 %6, %11, %13 offen lds\n\tbuffer_load_dwordx4 %7, %11, %13 offen offset:1024 lds\n\t"
                     "buffer_load_dwordx4 %8, %11, %13 offen offset:2048 lds\n\tbuffer_load_dwordx4 %9, %11, %13 offen offset:3072 lds"
                     :: "s"(base), "s"(base + 16384), "v"(vx[0]), "v"(vx[1]), "v"(vx[2]), "v"(vx[3]), "v"(vw[0]), "v"(vw[1]), "v"(vw[2]), "v"(vw[3]),
                        "s"(sx), "s"(sw), "s"(kt * 128), "s"(kt * 64 * (N3 * 2)) : "memory");
    };
    issue(0); issue(1); issue(2);
#pragma unroll 1
    for (int k = 0; k < 20; ++k) {
        const int nt_ = PF_XW ? (k == 0 ? 0 : k == 1 || k == 12 ? 1 : k < 12 ? 2 : 0) : 0;
        if (k < 18) {
            if (nt_ == 0) asm volatile("s_waitcnt vmcnt(16)\n\ts_barrier" ::: "memory");
            else if (nt_ == 1) asm volatile("s_waitcnt vmcnt(17)\n\ts_barrier" ::: "memory");
            else asm volatile("s_waitcnt vmcnt(18)\n\ts_barrier" ::: "memory");
        }
        else if (k == 18) asm volatile("s_waitcnt vmcnt(8)\n\ts_barrier" ::: "memory");
        else asm volatile("s_waitcnt vmcnt(0)\n\ts_barrier" ::: "memory");
        if (k == 0) STAMP(1);
        if (k == 10) STAMP(2);
        if (k + 3 < 20) issue(k + 3);
        if (k == 14 && lw == 0) { tag = read_tag(tagw); publish(ws, SY_GB + blockIdx.x * 64, tag); }
    }
}

DI void slow_block(const char* __restrict__ qblk, const char* __restrict__ kvh, int t0, int t1, int win, int q0, float (&o)[12]) {
    const int lane = threadIdx.x & 63, l31 = lane & 31, h2 = lane >> 5;
    __builtin_amdgcn_fence(__ATOMIC_ACQUIRE, "agent");
    const bf16x8 q16 = *(const bf16x8*)(qblk + lane * 16);
    s16x4 q8 = {0, 0, 0, 0}; if (h2 == 0) q8 = *(const s16x4*)(qblk + 1024 + l31 * 8);
    f32x16 acc = {}; float m = -3.0e38f;
    for (int t = t0; t < t1; ++t) {
        const char* kt = kvh + (size_t)t * KVT;
        const bf16x8 k16 = *(const bf16x8*)(kt + lane * 16);
        s16x4 k8 = {0, 0, 0, 0}; if (h2 == 0) k8 = *(const s16x4*)(kt + 1024 + l31 * 8);
        bf16x8 v0 = {}, v1 = {};
        if (l31 <= 20) { v0 = *(const bf16x8*)(kt + 1296 + (h2 * 21 + l31) * 16); v1 = *(const bf16x8*)(kt + 1968 + (h2 * 21 + l31) * 16); }
        f32x16 s = MFMA32(k16, q16, f32x16{});
        s = MFMA32K8(k8, q8, s);
        float tm = -3.0e38f;
#pragma unroll
        for (int r = 0; r < 16; ++r) {
            if (win) { const int dq = (q0 + l31) - (t * 32 + (r & 3) + 8 * (r >> 2) + 4 * h2); if ((unsigned)dq >= (unsigned)win) s[r] = -3.0e38f; }
            tm = fmaxf(tm, s[r]);
        }
        tm = fmaxf(tm, __shfl_xor(tm, 32));
        const float mn = fmaxf(m, tm), al = __builtin_amdgcn_exp2f(m - mn);
        m = mn;
        unsigned p[8];
#pragma unroll
        for (int r = 0; r < 16; r += 2) {
            const float e0 = s[r] <= -3.0e38f ? 0.f : __builtin_amdgcn_exp2f(s[r] - mn), e1 = s[r + 1] <= -3.0e38f ? 0.f : __builtin_amdgcn_exp2f(s[r + 1] - mn);
            p[r >> 1] = cvtpk(e0, e1);
        }
#pragma unroll
        for (int r = 0; r < 16; ++r) acc[r] *= al;
        const u32x4 p0 = {p[0], p[1], p[2], p[3]}, p1 = {p[4], p[5], p[6], p[7]};
        acc = MFMA32(v0, __builtin_bit_cast(bf16x8, p0), acc);
        acc = MFMA32(v1, __builtin_bit_cast(bf16x8, p1), acc);
    }
#pragma unroll
    for (int i = 0; i < 12; ++i) o[i] = acc[i];
}

DI void finish_block(float (&o)[12], const char* qblk, const char* kvh, int t0, int t1, int win, int q0, rsrc_t ro, unsigned dst) {
    const int lane = threadIdx.x & 63, l31 = lane & 31, h2 = lane >> 5;
    auto rowsum = [&]() { const auto s = __builtin_amdgcn_permlane32_swap(__float_as_uint(o[8]), __float_as_uint(o[8]), false, false); return __uint_as_float(s[1]); };
    float l = rowsum();
    bool bad = !(l < 1.0e36f) || !(l > 1.0e-30f);
#pragma unroll
    for (int i = 0; i < 12; ++i) bad = bad || !(fabsf(o[i]) < 3.0e38f);
    if (__any(bad)) { slow_block(qblk, kvh, t0, t1, win, q0, o); l = rowsum(); }
    const float r = __builtin_amdgcn_rcpf(l);
    const u32x4 a = {cvtpk(o[0] * r, o[1] * r), cvtpk(o[2] * r, o[3] * r), cvtpk(o[4] * r, o[5] * r), cvtpk(o[6] * r, o[7] * r)};
    const u32x2 b = {cvtpk(o[8] * r, o[9] * r), cvtpk(o[10] * r, o[11] * r)};
    st16(ro, dst + lane * 16, a);
    st8(ro, h2 == 0 ? dst + 1024 + l31 * 8 : OOB, b);
}

__global__ __launch_bounds__(512, 2) void mha2_fused(const char* __restrict__ xA, const char* __restrict__ wA, const char* __restrict__ woA,
                                                    const char* __restrict__ xB, const char* __restrict__ wB, const char* __restrict__ woB,
                                                    char* __restrict__ ws, char* __restrict__ out) {
    extern __shared__ __attribute__((aligned(16))) char smem[];
    asm volatile("ktext_begin_lbl:");
    const int tid = threadIdx.x, lane = tid & 63, wid = rfl(tid >> 6), l31 = lane & 31, h2 = lane >> 5;
    const int xcd = blockIdx.x & 7, wi = blockIdx.x >> 3;
    const unsigned ldsb = (unsigned)(uintptr_t)smem;
    STAMP(0);
    LDSP char* tagw = (LDSP char*)smem + LDS_BYTES - 8;
    u64 tag = 0;
    if (wid == 0) {
        const u64 e = __hip_atomic_load((const GAS u64*)(ws + WS_SYNC), __ATOMIC_RELAXED, __HIP_MEMORY_SCOPE_AGENT);
        *(volatile LDSP u64*)tagw = e + 1;
        asm volatile("s_waitcnt lgkmcnt(0)" ::: "memory");
    }
    if (wid == 2 || wid == 3) {
        const int t = tid - 128, e = blockIdx.x * 16 + t / 5, p = t % 5;
        const u32x4 one = {0x00003f80u, 0u, 0x00003f80u, 0u}, ones = {0x3f803f80u, 0x3f803f80u, 0x3f803f80u, 0x3f803f80u};
        st16(mk_rsrc(ws + WS_KV, 2 * KV_BYTES), t < 80 ? e * KVT + (p == 0 ? 1280 : 1296 + ((p - 1) >> 1) * 672 + (((p - 1) & 1) * 21 + 20) * 16) : OOB, p == 0 ? one : ones);
    }
    const int mt = (xcd >> 1) * 4 + (wi & 3), nt = (xcd & 1) * 8 + (wi >> 2);
    if (nt < 15) {
        if (wid < 4) {
            if (wid == 1 && wi < 2) {
                u64 pc; unsigned d, n;
                asm volatile("s_getpc_b64 %0\nLpc_%=:\n\ts_mov_b32 %1, Lpc_%=-ktext_begin_lbl\n\ts_mov_b32 %2, ktext_end_lbl-ktext_begin_lbl" : "=&s"(pc), "=&s"(d), "=&s"(n));
                const u32x4 sc = make_srd((const void*)(pc - d), n);
#pragma unroll
                for (int k = 0; k < 20; ++k) dma16(ldsb + 131072 + 1024 * (k & 7), (wi * 20 + k) * 1024 + lane * 16, sc, 0);
            }
            g_consumer(xA, wA, xB, wB, mt, nt, ldsb, ws);
        } else g_loader(xA, wA, xB, wB, mt, nt, ldsb, ws, tagw, tag);
    } else {
        unsigned acc = 0;
        const char* wo = (wi & 1) ? woB : woA;
        const unsigned part = (xcd >> 1) * 2 + ((wi >> 1) & 1), per = DM * DM * 2 / 8;
#pragma unroll 4
        for (unsigned o = part * per + tid * 16; o < (part + 1) * per; o += 8192) { const u32x4 v = *(const u32x4*)(wo + o); acc ^= v[0]; }
        asm volatile("" :: "v"(acc));
        asm volatile("s_waitcnt vmcnt(0)\n\ts_barrier" ::: "memory");
        if (wid == 4) { tag = read_tag(tagw); publish(ws, SY_GB + blockIdx.x * 64, tag); }
    }
    if (wid == 4) {
        wait_flags<4>(ws, SY_GB, 64, tag);
        if (blockIdx.x == 0 && lane == 0) __hip_atomic_store((GAS u64*)(ws + WS_SYNC), tag, __ATOMIC_RELAXED, __HIP_MEMORY_SCOPE_AGENT);
    }
    if (nt >= 15 || wid >= 4) asm volatile("s_barrier" ::: "memory");
    STAMP(3);
    const int head = xcd * 4 + (wi & 3), j = wi >> 2;
    const int qb = 8 * j + wid;
    const u32x4 srdf = make_srd(ws + WS_SYNC, SY_BYTES);
    f32x16 ob, cdiag, cfar; float negm; u32x4 qa16; u32x2 qa8;
    {
        const int g = wid >> 2, kq = wid & 3;
        const u32x4 srdq = make_srd(ws + WS_QF, 2 * QF_BYTES), srdkv = make_srd(ws + WS_KV, 2 * KV_BYTES);
        const unsigned vk16 = lane * 16, vk8 = h2 == 0 ? 1024 + l31 * 8 : 1280, vq8 = h2 == 0 ? l31 * 8 : OOB;
        const unsigned vv = l31 <= 20 ? (h2 * 21 + l31) * 16 : OOB;
        const unsigned sqb = QF_BYTES + (head * 64 + 8 * j + 4 * g) * QT;
        const unsigned skv = KV_BYTES + (head * 64 + kq * 16) * KVT;
        const unsigned sqa = (head * 64 + qb) * QT;
        const int ta = 8 * j - 8 + 2 * wid;
        const unsigned skva = ta < 0 ? OOB : (unsigned)(head * 64 + ta) * KVT, sm0 = ldsb + LDS_AIMG + wid * 7168;
        const unsigned pb = ldsb + LDS_PART + lane * 16;
        const unsigned vp1 = pb + ((g * 4 + ((kq + 1) & 3)) * 3 + 0) * 4096, vp2 = pb + ((g * 4 + ((kq + 2) & 3)) * 3 + 1) * 4096, vp3 = pb + ((g * 4 + ((kq + 3) & 3)) * 3 + 2) * 4096;
        const unsigned vpub = (wid == 4 && lane == 0) ? SY_GA + blockIdx.x * 64 : OOB, vpoll = wid == 4 ? SY_GA + lane * 64 : OOB;
        asm volatile(
        "s_nop 4\n\t"
        "s_add_u32 s74, %[skq], 0\n\t"
        "s_and_b32 s74, s74, 3\n\t"
        "s_mul_i32 s74, s74, 1280\n\t"
        "s_add_u32 s74, s74, %[sqb]\n\t"
        "s_add_u32 s75, %[skq], 1\n\t"
        "s_and_b32 s75, s75, 3\n\t"
        "s_mul_i32 s75, s75, 1280\n\t"
        "s_add_u32 s75, s75, %[sqb]\n\t"
        "s_add_u32 s76, %[skq], 2\n\t"
        "s_and_b32 s76, s76, 3\n\t"
        "s_mul_i32 s76, s76, 1280\n\t"
        "s_add_u32 s76, s76, %[sqb]\n\t"
        "s_add_u32 s77, %[skq], 3\n\t"
        "s_and_b32 s77, s77, 3\n\t"
        "s_mul_i32 s77, s77, 1280\n\t"
        "s_add_u32 s77, s77, %[sqb]\n\t"
        "buffer_load_dwordx4 v[32:35], %[vk16], %[srdq], s74 offen sc1\n\t"
        "buffer_load_dwordx4 v[38:41], %[vk16], %[srdq], s75 offen sc1\n\t"
        "buffer_load_dwordx4 v[44:47], %[vk16], %[srdq], s76 offen sc1\n\t"
        "buffer_load_dwordx4 v[50:53], %[vk16], %[srdq], s77 offen sc1\n\t"
        "s_mov_b32 s60, %[skv]\n\t"
        "s_add_u32 s61, s60, 2640\n\t"
        "buffer_load_dwordx4 v[160:163], %[vk16], %[srdkv], s60 offen sc1\n\t"
        "buffer_load_dwordx2 v[36:37], %[vq8], %[srdq], s74 offen offset:1024 sc1\n\t"
        "buffer_load_dwordx2 v[42:43], %[vq8], %[srdq], s75 offen offset:1024 sc1\n\t"
        "buffer_load_dwordx2 v[48:49], %[vq8], %[srdq], s76 offen offset:1024 sc1\n\t"
        "buffer_load_dwordx2 v[54:55], %[vq8], %[srdq], s77 offen offset:1024 sc1\n\t"
        "buffer_load_dwordx2 v[164:165], %[vk8], %[srdkv], s60 offen sc1\n\t"
        "buffer_load_dwordx4 v[166:169], %[vv], %[srdkv], s60 offen offset:1296 sc1\n\t"
        "buffer_load_dwordx4 v[170:173], %[vv], %[srdkv], s60 offen offset:1968 sc1\n\t"
        "buffer_load_dwordx4 v[174:177], %[vk16], %[srdkv], s61 offen sc1\n\t"
        "buffer_load_dwordx2 v[178:179], %[vk8], %[srdkv], s61 offen sc1\n\t"
        "buffer_load_dwordx4 v[180:183], %[vv], %[srdkv], s61 offen offset:1296 sc1\n\t"
        "s_setprio 3\n\t"
        "s_mov_b32 s62, 0\n\t"
        "s_mov_b32 s63, -1\n\t"
        "v_lshlrev_b32 v120, 4, %[vl31]\n\t"
        "v_cmp_ge_u32 vcc, 20, %[vl31]\n\t"
        "v_add_u32 v123, 1024, v120\n\t"
        "v_mov_b32 v122, 0x40000000\n\t"
        "v_cndmask_b32 v121, v122, v120, vcc\n\t"
        "v_cmp_gt_u32 vcc, 16, %[vl31]\n\t"
        "v_mov_b32 v122, 0x500\n\t"
        "s_nop 0\n\t"
        "v_cndmask_b32 v122, v122, v123, vcc\n\t"
        "v_add_u32 v123, 1296, v121\n\t"
        "v_cndmask_b32 v240, v122, v123, s[62:63]\n\t"
        "v_add_u32 v123, 1968, v121\n\t"
        "v_add_u32 v241, 1632, v121\n\t"
        "v_cndmask_b32 v241, v241, v123, s[62:63]\n\t"
        "v_add_u32 v123, 2640, v120\n\t"
        "v_add_u32 v242, 2304, v121\n\t"
        "v_cndmask_b32 v242, v242, v123, s[62:63]\n\t"
        "v_add_u32 v123, 2640, v122\n\t"
        "v_add_u32 v243, 3152, v120\n\t"
        "v_cndmask_b32 v243, v243, v123, s[62:63]\n\t"
        "v_subrev_u32 v123, 4, %[vl31]\n\t"
        "v_cndmask_b32 v123, %[vl31], v123, s[62:63]\n\t"
        "v_cvt_f32_i32 v123, v123\n\t"
        "v_mul_f32 v123, 0x71800000, v123\n\t"
        "v_subrev_f32 v120, 0xf1000000, v123\n\t"
        "v_min_f32 v194, 0, v120\n\t"
        "v_min_f32_e64 v210, -v120, 0\n\t"
        "v_subrev_f32 v120, 0x71000000, v123\n\t"
        "v_min_f32 v195, 0, v120\n\t"
        "v_min_f32_e64 v211, -v120, 0\n\t"
        "v_subrev_f32 v120, 0x71c00000, v123\n\t"
        "v_min_f32 v196, 0, v120\n\t"
        "v_min_f32_e64 v212, -v120, 0\n\t"
        "v_subrev_f32 v120, 0x72200000, v123\n\t"
        "v_min_f32 v197, 0, v120\n\t"
        "v_min_f32_e64 v213, -v120, 0\n\t"
        "v_subrev_f32 v120, 0x72f00000, v123\n\t"
        "v_min_f32 v198, 0, v120\n\t"
        "v_min_f32_e64 v214, -v120, 0\n\t"
        "v_subrev_f32 v120, 0x73080000, v123\n\t"
        "v_min_f32 v199, 0, v120\n\t"
        "v_min_f32_e64 v215, -v120, 0\n\t"
        "v_subrev_f32 v120, 0x73180000, v123\n\t"
        "v_min_f32 v200, 0, v120\n\t"
        "v_min_f32_e64 v216, -v120, 0\n\t"
        "v_subrev_f32 v120, 0x73280000, v123\n\t"
        "v_min_f32 v201, 0, v120\n\t"
        "v_min_f32_e64 v217, -v120, 0\n\t"
        "v_subrev_f32 v120, 0x73780000, v123\n\t"
        "v_min_f32 v202, 0, v120\n\t"
        "v_min_f32_e64 v218, -v120, 0\n\t"
        "v_subrev_f32 v120, 0x73840000, v123\n\t"
        "v_min_f32 v203, 0, v120\n\t"
        "v_min_f32_e64 v219, -v120, 0\n\t"
        "v_subrev_f32 v120, 0x738c0000, v123\n\t"
        "v_min_f32 v204, 0, v120\n\t"
        "v_min_f32_e64 v220, -v120, 0\n\t"
        "v_subrev_f32 v120, 0x73940000, v123\n\t"
        "v_min_f32 v205, 0, v120\n\t"
        "v_min_f32_e64 v221, -v120, 0\n\t"
        "v_subrev_f32 v120, 0x73bc0000, v123\n\t"
        "v_min_f32 v206, 0, v120\n\t"
        "v_min_f32_e64 v222, -v120, 0\n\t"
        "v_subrev_f32 v120, 0x73c40000, v123\n\t"
        "v_min_f32 v207, 0, v120\n\t"
        "v_min_f32_e64 v223, -v120, 0\n\t"
        "v_subrev_f32 v120, 0x73cc0000, v123\n\t"
        "v_min_f32 v208, 0, v120\n\t"
        "v_min_f32_e64 v224, -v120, 0\n\t"
        "v_subrev_f32 v120, 0x73d40000, v123\n\t"
        "v_min_f32 v209, 0, v120\n\t"
        "v_min_f32_e64 v225, -v120, 0\n\t"
        "s_waitcnt vmcnt(10)\n\t"
        "v_mfma_f32_32x32x16_bf16 v[56:71], v[160:163], v[32:35], 0\n\t"
        "v_mfma_f32_32x32x16_bf16 v[72:87], v[160:163], v[38:41], 0\n\t"
        "v_mfma_f32_32x32x16_bf16 v[88:103], v[160:163], v[44:47], 0\n\t"
        "v_mfma_f32_32x32x16_bf16 v[104:119], v[160:163], v[50:53], 0\n\t"
        "s_waitcnt vmcnt(5)\n\t"
        "v_mfma_f32_32x32x8_bf16 v[56:71], v[164:165], v[36:37], v[56:71]\n\t"
        "v_mfma_f32_32x32x8_bf16 v[72:87], v[164:165], v[42:43], v[72:87]\n\t"
        "v_mfma_f32_32x32x8_bf16 v[88:103], v[164:165], v[48:49], v[88:103]\n\t"
        "v_mfma_f32_32x32x8_bf16 v[104:119], v[164:165], v[54:55], v[104:119]\n\t"
        "s_nop 7\n\t"
        "s_nop 0\n\t"
        "v_max3_f32 v192, v56, v57, v58\n\t"
        "v_max3_f32 v192, v192, v59, v60\n\t"
        "v_max3_f32 v192, v192, v61, v62\n\t"
        "v_max3_f32 v192, v192, v63, v64\n\t"
        "v_max3_f32 v192, v192, v65, v66\n\t"
        "v_max3_f32 v192, v192, v67, v68\n\t"
        "v_max3_f32 v192, v192, v69, v70\n\t"
        "v_max_f32 v192, v192, v71\n\t"
        "v_mov_b32 v193, v192\n\t"
        "s_nop 1\n\t"
        "v_permlane32_swap_b32 v192, v193\n\t"
        "s_nop 1\n\t"
        "v_max_f32 v192, v192, v193\n\t"
        "v_cvt_pk_bf16_f32 v193, -v192, 0\n\t"
        "v_cndmask_b32 v36, v36, v193, s[62:63]\n\t"
        "v_lshlrev_b32 v188, 16, v193\n\t"
        "s_nop 0\n\t"
        "v_mfma_f32_32x32x16_bf16 v[120:135], v[160:163], v[32:35], 0\n\t"
        "v_mfma_f32_32x32x8_bf16 v[120:135], v[164:165], v[36:37], v[120:135]\n\t"
        "v_max3_f32 v192, v72, v73, v74\n\t"
        "v_max3_f32 v192, v192, v75, v76\n\t"
        "v_max3_f32 v192, v192, v77, v78\n\t"
        "v_max3_f32 v192, v192, v79, v80\n\t"
        "v_max3_f32 v192, v192, v81, v82\n\t"
        "v_max3_f32 v192, v192, v83, v84\n\t"
        "v_max3_f32 v192, v192, v85, v86\n\t"
        "v_max_f32 v192, v192, v87\n\t"
        "v_mov_b32 v193, v192\n\t"
        "s_nop 1\n\t"
        "v_permlane32_swap_b32 v192, v193\n\t"
        "s_nop 1\n\t"
        "v_max_f32 v192, v192, v193\n\t"
        "v_cvt_pk_bf16_f32 v193, -v192, 0\n\t"
        "v_cndmask_b32 v42, v42, v193, s[62:63]\n\t"
        "v_lshlrev_b32 v189, 16, v193\n\t"
        "v_max3_f32 v192, v88, v89, v90\n\t"
        "v_max3_f32 v192, v192, v91, v92\n\t"
        "v_max3_f32 v192, v192, v93, v94\n\t"
        "v_max3_f32 v192, v192, v95, v96\n\t"
        "v_max3_f32 v192, v192, v97, v98\n\t"
        "v_max3_f32 v192, v192, v99, v100\n\t"
        "v_max3_f32 v192, v192, v101, v102\n\t"
        "v_max_f32 v192, v192, v103\n\t"
        "v_mov_b32 v193, v192\n\t"
        "s_nop 1\n\t"
        "v_permlane32_swap_b32 v192, v193\n\t"
        "s_nop 1\n\t"
        "v_max_f32 v192, v192, v193\n\t"
        "v_cvt_pk_bf16_f32 v193, -v192, 0\n\t"
        "v_cndmask_b32 v48, v48, v193, s[62:63]\n\t"
        "v_lshlrev_b32 v190, 16, v193\n\t"
        "v_max3_f32 v192, v104, v105, v106\n\t"
        "v_max3_f32 v192, v192, v107, v108\n\t"
        "v_max3_f32 v192, v192, v109, v110\n\t"
        "v_max3_f32 v192, v192, v111, v112\n\t"
        "v_max3_f32 v192, v192, v113, v114\n\t"
        "v_max3_f32 v192, v192, v115, v116\n\t"
        "v_max3_f32 v192, v192, v117, v118\n\t"
        "v_max_f32 v192, v192, v119\n\t"
        "v_mov_b32 v193, v192\n\t"
        "s_nop 1\n\t"
        "v_permlane32_swap_b32 v192, v193\n\t"
        "s_nop 1\n\t"
        "v_max_f32 v192, v192, v193\n\t"
        "v_cvt_pk_bf16_f32 v193, -v192, 0\n\t"
        "v_cndmask_b32 v54, v54, v193, s[62:63]\n\t"
        "v_lshlrev_b32 v191, 16, v193\n\t"
        "s_mov_b32 s67, 4096\n\t"
        "s_mov_b32 s68, 8192\n\t"
        "s_mov_b32 s69, 12288\n\t"
        "v_mfma_f32_32x32x16_bf16 v[136:151], v[160:163], v[38:41], 0\n\t"
        "v_exp_f32 v120, v120\n\t"
        "v_exp_f32 v121, v121\n\t"
        "v_exp_f32 v122, v122\n\t"
        "v_exp_f32 v123, v123\n\t"
        "v_cvt_pk_bf16_f32 v152, v120, v121\n\t"
        "v_cvt_pk_bf16_f32 v153, v122, v123\n\t"
        "v_exp_f32 v124, v124\n\t"
        "v_exp_f32 v125, v125\n\t"
        "v_exp_f32 v126, v126\n\t"
        "v_exp_f32 v127, v127\n\t"
        "v_cvt_pk_bf16_f32 v154, v124, v125\n\t"
        "v_cvt_pk_bf16_f32 v155, v126, v127\n\t"
        "v_mfma_f32_32x32x8_bf16 v[136:151], v[164:165], v[42:43], v[136:151]\n\t"
        "v_exp_f32 v128, v128\n\t"
        "v_exp_f32 v129, v129\n\t"
        "v_exp_f32 v130, v130\n\t"
        "v_exp_f32 v131, v131\n\t"
        "v_cvt_pk_bf16_f32 v156, v128, v129\n\t"
        "v_cvt_pk_bf16_f32 v157, v130, v131\n\t"
        "s_waitcnt vmcnt(4)\n\t"
        "v_mfma_f32_32x32x16_bf16 v[56:71], v[166:169], v[152:155], 0\n\t"
        "v_exp_f32 v132, v132\n\t"
        "v_exp_f32 v133, v133\n\t"
        "v_exp_f32 v134, v134\n\t"
        "v_exp_f32 v135, v135\n\t"
        "v_cvt_pk_bf16_f32 v158, v132, v133\n\t"
        "v_cvt_pk_bf16_f32 v159, v134, v135\n\t"
        "v_mfma_f32_32x32x16_bf16 v[120:135], v[160:163], v[44:47], 0\n\t"
        "buffer_load_dwordx4 v[184:187], %[vv], %[srdkv], s61 offen offset:1968 sc1\n\t"
        "v_exp_f32 v136, v136\n\t"
        "v_exp_f32 v137, v137\n\t"
        "v_exp_f32 v138, v138\n\t"
        "v_exp_f32 v139, v139\n\t"
        "v_cvt_pk_bf16_f32 v152, v136, v137\n\t"
        "v_cvt_pk_bf16_f32 v153, v138, v139\n\t"
        "s_waitcnt vmcnt(4)\n\t"
        "v_mfma_f32_32x32x16_bf16 v[56:71], v[170:173], v[156:159], v[56:71]\n\t"
        "v_exp_f32 v140, v140\n\t"
        "v_exp_f32 v141, v141\n\t"
        "v_exp_f32 v142, v142\n\t"
        "v_exp_f32 v143, v143\n\t"
        "v_cvt_pk_bf16_f32 v154, v140, v141\n\t"
        "v_cvt_pk_bf16_f32 v155, v142, v143\n\t"
        "v_mfma_f32_32x32x8_bf16 v[120:135], v[164:165], v[48:49], v[120:135]\n\t"
        "v_exp_f32 v144, v144\n\t"
        "v_exp_f32 v145, v145\n\t"
        "v_exp_f32 v146, v146\n\t"
        "v_exp_f32 v147, v147\n\t"
        "v_cvt_pk_bf16_f32 v156, v144, v145\n\t"
        "v_cvt_pk_bf16_f32 v157, v146, v147\n\t"
        "v_mfma_f32_32x32x16_bf16 v[72:87], v[166:169], v[152:155], 0\n\t"
        "v_exp_f32 v148, v148\n\t"
        "v_exp_f32 v149, v149\n\t"
        "v_exp_f32 v150, v150\n\t"
        "v_exp_f32 v151, v151\n\t"
        "v_cvt_pk_bf16_f32 v158, v148, v149\n\t"
        "v_cvt_pk_bf16_f32 v159, v150, v151\n\t"
        "v_mfma_f32_32x32x16_bf16 v[136:151], v[160:163], v[50:53], 0\n\t"
        "v_exp_f32 v120, v120\n\t"
        "v_exp_f32 v121, v121\n\t"
        "v_exp_f32 v122, v122\n\t"
        "v_exp_f32 v123, v123\n\t"
        "v_cvt_pk_bf16_f32 v152, v120, v121\n\t"
        "v_cvt_pk_bf16_f32 v153, v122, v123\n\t"
        "v_mfma_f32_32x32x16_bf16 v[72:87], v[170:173], v[156:159], v[72:87]\n\t"
        "v_exp_f32 v124, v124\n\t"
        "v_exp_f32 v125, v125\n\t"
        "v_exp_f32 v126, v126\n\t"
        "v_exp_f32 v127, v127\n\t"
        "v_cvt_pk_bf16_f32 v154, v124, v125\n\t"
        "v_cvt_pk_bf16_f32 v155, v126, v127\n\t"
        "v_mfma_f32_32x32x8_bf16 v[136:151], v[164:165], v[54:55], v[136:151]\n\t"
        "v_exp_f32 v128, v128\n\t"
        "v_exp_f32 v129, v129\n\t"
        "v_exp_f32 v130, v130\n\t"
        "v_exp_f32 v131, v131\n\t"
        "v_cvt_pk_bf16_f32 v156, v128, v129\n\t"
        "v_cvt_pk_bf16_f32 v157, v130, v131\n\t"
        "v_mfma_f32_32x32x16_bf16 v[88:103], v[166:169], v[152:155], 0\n\t"
        "v_exp_f32 v132, v132\n\t"
        "v_exp_f32 v133, v133\n\t"
        "v_exp_f32 v134, v134\n\t"
        "v_exp_f32 v135, v135\n\t"
        "v_cvt_pk_bf16_f32 v158, v132, v133\n\t"
        "v_cvt_pk_bf16_f32 v159, v134, v135\n\t"
        "s_waitcnt vmcnt(3)\n\t"
        "v_mfma_f32_32x32x16_bf16 v[120:135], v[174:177], v[32:35], 0\n\t"
        "s_add_u32 s60, s60, 5280\n\t"
        "buffer_load_dwordx4 v[160:163], %[vk16], %[srdkv], s60 offen sc1\n\t"
        "v_exp_f32 v136, v136\n\t"
        "v_exp_f32 v137, v137\n\t"
        "v_exp_f32 v138, v138\n\t"
        "v_exp_f32 v139, v139\n\t"
        "v_cvt_pk_bf16_f32 v152, v136, v137\n\t"
        "v_cvt_pk_bf16_f32 v153, v138, v139\n\t"
        "v_mfma_f32_32x32x16_bf16 v[88:103], v[170:173], v[156:159], v[88:103]\n\t"
        "v_exp_f32 v140, v140\n\t"
        "v_exp_f32 v141, v141\n\t"
        "v_exp_f32 v142, v142\n\t"
        "v_exp_f32 v143, v143\n\t"
        "v_cvt_pk_bf16_f32 v154, v140, v141\n\t"
        "v_cvt_pk_bf16_f32 v155, v142, v143\n\t"
        "s_waitcnt vmcnt(3)\n\t"
        "v_mfma_f32_32x32x8_bf16 v[120:135], v[178:179], v[36:37], v[120:135]\n\t"
        "buffer_load_dwordx2 v[164:165], %[vk8], %[srdkv], s60 offen sc1\n\t"
        "v_exp_f32 v144, v144\n\t"
        "v_exp_f32 v145, v145\n\t"
        "v_exp_f32 v146, v146\n\t"
        "v_exp_f32 v147, v147\n\t"
        "v_cvt_pk_bf16_f32 v156, v144, v145\n\t"
        "v_cvt_pk_bf16_f32 v157, v146, v147\n\t"
        "v_mfma_f32_32x32x16_bf16 v[104:119], v[166:169], v[152:155], 0\n\t"
        "buffer_load_dwordx4 v[166:169], %[vv], %[srdkv], s60 offen offset:1296 sc1\n\t"
        "v_exp_f32 v148, v148\n\t"
        "v_exp_f32 v149, v149\n\t"
        "v_exp_f32 v150, v150\n\t"
        "v_exp_f32 v151, v151\n\t"
        "v_cvt_pk_bf16_f32 v158, v148, v149\n\t"
        "v_cvt_pk_bf16_f32 v159, v150, v151\n\t"
        "v_mfma_f32_32x32x16_bf16 v[136:151], v[174:177], v[38:41], 0\n\t"
        "v_exp_f32 v120, v120\n\t"
        "v_exp_f32 v121, v121\n\t"
        "v_exp_f32 v122, v122\n\t"
        "v_exp_f32 v123, v123\n\t"
        "v_cvt_pk_bf16_f32 v152, v120, v121\n\t"
        "v_cvt_pk_bf16_f32 v153, v122, v123\n\t"
        "v_mfma_f32_32x32x16_bf16 v[104:119], v[170:173], v[156:159], v[104:119]\n\t"
        "v_exp_f32 v124, v124\n\t"
        "v_exp_f32 v125, v125\n\t"
        "v_exp_f32 v126, v126\n\t"
        "v_exp_f32 v127, v127\n\t"
        "v_cvt_pk_bf16_f32 v154, v124, v125\n\t"
        "v_cvt_pk_bf16_f32 v155, v126, v127\n\t"
        "v_mfma_f32_32x32x8_bf16 v[136:151], v[178:179], v[42:43], v[136:151]\n\t"
        "v_exp_f32 v128, v128\n\t"
        "v_exp_f32 v129, v129\n\t"
        "v_exp_f32 v130, v130\n\t"
        "v_exp_f32 v131, v131\n\t"
        "v_cvt_pk_bf16_f32 v156, v128, v129\n\t"
        "v_cvt_pk_bf16_f32 v157, v130, v131\n\t"
        "s_waitcnt vmcnt(4)\n\t"
        "v_mfma_f32_32x32x16_bf16 v[56:71], v[180:183], v[152:155], v[56:71]\n\t"
        "v_exp_f32 v132, v132\n\t"
        "v_exp_f32 v133, v133\n\t"
        "v_exp_f32 v134, v134\n\t"
        "v_exp_f32 v135, v135\n\t"
        "v_cvt_pk_bf16_f32 v158, v132, v133\n\t"
        "v_cvt_pk_bf16_f32 v159, v134, v135\n\t"
        "v_mfma_f32_32x32x16_bf16 v[120:135], v[174:177], v[44:47], 0\n\t"
        "buffer_load_dwordx4 v[170:173], %[vv], %[srdkv], s60 offen offset:1968 sc1\n\t"
        "v_exp_f32 v136, v136\n\t"
        "v_exp_f32 v137, v137\n\t"
        "v_exp_f32 v138, v138\n\t"
        "v_exp_f32 v139, v139\n\t"
        "v_cvt_pk_bf16_f32 v152, v136, v137\n\t"
        "v_cvt_pk_bf16_f32 v153, v138, v139\n\t"
        "s_waitcnt vmcnt(4)\n\t"
        "v_mfma_f32_32x32x16_bf16 v[56:71], v[184:187], v[156:159], v[56:71]\n\t"
        "s_barrier\n\t"
        "v_mov_b32 v192, s72\n\t"
        "v_mov_b32 v193, s73\n\t"
        "s_nop 0\n\t"
        "buffer_store_dwordx2 v[192:193], %[vpub], %[srdf], 0 offen sc1\n\t"
        "v_exp_f32 v140, v140\n\t"
        "v_exp_f32 v141, v141\n\t"
        "v_exp_f32 v142, v142\n\t"
        "v_exp_f32 v143, v143\n\t"
        "v_cvt_pk_bf16_f32 v154, v140, v141\n\t"
        "v_cvt_pk_bf16_f32 v155, v142, v143\n\t"
        "v_mfma_f32_32x32x8_bf16 v[120:135], v[178:179], v[48:49], v[120:135]\n\t"
        "v_exp_f32 v144, v144\n\t"
        "v_exp_f32 v145, v145\n\t"
        "v_exp_f32 v146, v146\n\t"
        "v_exp_f32 v147, v147\n\t"
        "v_cvt_pk_bf16_f32 v156, v144, v145\n\t"
        "v_cvt_pk_bf16_f32 v157, v146, v147\n\t"
        "v_mfma_f32_32x32x16_bf16 v[72:87], v[180:183], v[152:155], v[72:87]\n\t"
        "v_exp_f32 v148, v148\n\t"
        "v_exp_f32 v149, v149\n\t"
        "v_exp_f32 v150, v150\n\t"
        "v_exp_f32 v151, v151\n\t"
        "v_cvt_pk_bf16_f32 v158, v148, v149\n\t"
        "v_cvt_pk_bf16_f32 v159, v150, v151\n\t"
        "v_mfma_f32_32x32x16_bf16 v[136:151], v[174:177], v[50:53], 0\n\t"
        "v_exp_f32 v120, v120\n\t"
        "v_exp_f32 v121, v121\n\t"
        "v_exp_f32 v122, v122\n\t"
        "v_exp_f32 v123, v123\n\t"
        "v_cvt_pk_bf16_f32 v152, v120, v121\n\t"
        "v_cvt_pk_bf16_f32 v153, v122, v123\n\t"
        "v_mfma_f32_32x32x16_bf16 v[72:87], v[184:187], v[156:159], v[72:87]\n\t"
        "v_exp_f32 v124, v124\n\t"
        "v_exp_f32 v125, v125\n\t"
        "v_exp_f32 v126, v126\n\t"
        "v_exp_f32 v127, v127\n\t"
        "v_cvt_pk_bf16_f32 v154, v124, v125\n\t"
        "v_cvt_pk_bf16_f32 v155, v126, v127\n\t"
        "v_mfma_f32_32x32x8_bf16 v[136:151], v[178:179], v[54:55], v[136:151]\n\t"
        "v_exp_f32 v128, v128\n\t"
        "v_exp_f32 v129, v129\n\t"
        "v_exp_f32 v130, v130\n\t"
        "v_exp_f32 v131, v131\n\t"
        "v_cvt_pk_bf16_f32 v156, v128, v129\n\t"
        "v_cvt_pk_bf16_f32 v157, v130, v131\n\t"
        "v_mfma_f32_32x32x16_bf16 v[88:103], v[180:183], v[152:155], v[88:103]\n\t"
        "v_exp_f32 v132, v132\n\t"
        "v_exp_f32 v133, v133\n\t"
        "v_exp_f32 v134, v134\n\t"
        "v_exp_f32 v135, v135\n\t"
        "v_cvt_pk_bf16_f32 v158, v132, v133\n\t"
        "v_cvt_pk_bf16_f32 v159, v134, v135\n\t"
        "s_waitcnt vmcnt(4)\n\t"
        "v_mfma_f32_32x32x16_bf16 v[120:135], v[160:163], v[32:35], 0\n\t"
        "s_add_u32 s61, s61, 5280\n\t"
        "buffer_load_dwordx4 v[174:177], %[vk16], %[srdkv], s61 offen sc1\n\t"
        "v_exp_f32 v136, v136\n\t"
        "v_exp_f32 v137, v137\n\t"
        "v_exp_f32 v138, v138\n\t"
        "v_exp_f32 v139, v139\n\t"
        "v_cvt_pk_bf16_f32 v152, v136, v137\n\t"
        "v_cvt_pk_bf16_f32 v153, v138, v139\n\t"
        "v_mfma_f32_32x32x16_bf16 v[88:103], v[184:187], v[156:159], v[88:103]\n\t"
        "v_exp_f32 v140, v140\n\t"
        "v_exp_f32 v141, v141\n\t"
        "v_exp_f32 v142, v142\n\t"
        "v_exp_f32 v143, v143\n\t"
        "v_cvt_pk_bf16_f32 v154, v140, v141\n\t"
        "v_cvt_pk_bf16_f32 v155, v142, v143\n\t"
        "s_waitcnt vmcnt(4)\n\t"
        "v_mfma_f32_32x32x8_bf16 v[120:135], v[164:165], v[36:37], v[120:135]\n\t"
        "buffer_load_dwordx2 v[178:179], %[vk8], %[srdkv], s61 offen sc1\n\t"
        "v_exp_f32 v144, v144\n\t"
        "v_exp_f32 v145, v145\n\t"
        "v_exp_f32 v146, v146\n\t"
        "v_exp_f32 v147, v147\n\t"
        "v_cvt_pk_bf16_f32 v156, v144, v145\n\t"
        "v_cvt_pk_bf16_f32 v157, v146, v147\n\t"
        "v_mfma_f32_32x32x16_bf16 v[104:119], v[180:183], v[152:155], v[104:119]\n\t"
        "buffer_load_dwordx4 v[180:183], %[vv], %[srdkv], s61 offen offset:1296 sc1\n\t"
        "v_exp_f32 v148, v148\n\t"
        "v_exp_f32 v149, v149\n\t"
        "v_exp_f32 v150, v150\n\t"
        "v_exp_f32 v151, v151\n\t"
        "v_cvt_pk_bf16_f32 v158, v148, v149\n\t"
        "v_cvt_pk_bf16_f32 v159, v150, v151\n\t"
        "v_mfma_f32_32x32x16_bf16 v[136:151], v[160:163], v[38:41], 0\n\t"
        "v_exp_f32 v120, v120\n\t"
        "v_exp_f32 v121, v121\n\t"
        "v_exp_f32 v122, v122\n\t"
        "v_exp_f32 v123, v123\n\t"
        "v_cvt_pk_bf16_f32 v152, v120, v121\n\t"
        "v_cvt_pk_bf16_f32 v153, v122, v123\n\t"
        "v_mfma_f32_32x32x16_bf16 v[104:119], v[184:187], v[156:159], v[104:119]\n\t"
        "v_exp_f32 v124, v124\n\t"
        "v_exp_f32 v125, v125\n\t"
        "v_exp_f32 v126, v126\n\t"
        "v_exp_f32 v127, v127\n\t"
        "v_cvt_pk_bf16_f32 v154, v124, v125\n\t"
        "v_cvt_pk_bf16_f32 v155, v126, v127\n\t"
        "v_mfma_f32_32x32x8_bf16 v[136:151], v[164:165], v[42:43], v[136:151]\n\t"
        "v_exp_f32 v128, v128\n\t"
        "v_exp_f32 v129, v129\n\t"
        "v_exp_f32 v130, v130\n\t"
        "v_exp_f32 v131, v131\n\t"
        "v_cvt_pk_bf16_f32 v156, v128, v129\n\t"
        "v_cvt_pk_bf16_f32 v157, v130, v131\n\t"
        "s_waitcnt vmcnt(5)\n\t"
        "v_mfma_f32_32x32x16_bf16 v[56:71], v[166:169], v[152:155], v[56:71]\n\t"
        "v_exp_f32 v132, v132\n\t"
        "v_exp_f32 v133, v133\n\t"
        "v_exp_f32 v134, v134\n\t"
        "v_exp_f32 v135, v135\n\t"
        "v_cvt_pk_bf16_f32 v158, v132, v133\n\t"
        "v_cvt_pk_bf16_f32 v159, v134, v135\n\t"
        "v_mfma_f32_32x32x16_bf16 v[120:135], v[160:163], v[44:47], 0\n\t"
        "buffer_load_dwordx4 v[184:187], %[vv], %[srdkv], s61 offen offset:1968 sc1\n\t"
        "v_exp_f32 v136, v136\n\t"
        "v_exp_f32 v137, v137\n\t"
        "v_exp_f32 v138, v138\n\t"
        "v_exp_f32 v139, v139\n\t"
        "v_cvt_pk_bf16_f32 v152, v136, v137\n\t"
        "v_cvt_pk_bf16_f32 v153, v138, v139\n\t"
        "s_waitcnt vmcnt(5)\n\t"
        "v_mfma_f32_32x32x16_bf16 v[56:71], v[170:173], v[156:159], v[56:71]\n\t"
        "v_exp_f32 v140, v140\n\t"
        "v_exp_f32 v141, v141\n\t"
        "v_exp_f32 v142, v142\n\t"
        "v_exp_f32 v143, v143\n\t"
        "v_cvt_pk_bf16_f32 v154, v140, v141\n\t"
        "v_cvt_pk_bf16_f32 v155, v142, v143\n\t"
        "v_mfma_f32_32x32x8_bf16 v[120:135], v[164:165], v[48:49], v[120:135]\n\t"
        "v_exp_f32 v144, v144\n\t"
        "v_exp_f32 v145, v145\n\t"
        "v_exp_f32 v146, v146\n\t"
        "v_exp_f32 v147, v147\n\t"
        "v_cvt_pk_bf16_f32 v156, v144, v145\n\t"
        "v_cvt_pk_bf16_f32 v157, v146, v147\n\t"
        "v_mfma_f32_32x32x16_bf16 v[72:87], v[166:169], v[152:155], v[72:87]\n\t"
        "v_exp_f32 v148, v148\n\t"
        "v_exp_f32 v149, v149\n\t"
        "v_exp_f32 v150, v150\n\t"
        "v_exp_f32 v151, v151\n\t"
        "v_cvt_pk_bf16_f32 v158, v148, v149\n\t"
        "v_cvt_pk_bf16_f32 v159, v150, v151\n\t"
        "v_mfma_f32_32x32x16_bf16 v[136:151], v[160:163], v[50:53], 0\n\t"
        "v_exp_f32 v120, v120\n\t"
        "v_exp_f32 v121, v121\n\t"
        "v_exp_f32 v122, v122\n\t"
        "v_exp_f32 v123, v123\n\t"
        "v_cvt_pk_bf16_f32 v152, v120, v121\n\t"
        "v_cvt_pk_bf16_f32 v153, v122, v123\n\t"
        "v_mfma_f32_32x32x16_bf16 v[72:87], v[170:173], v[156:159], v[72:87]\n\t"
        "v_exp_f32 v124, v124\n\t"
        "v_exp_f32 v125, v125\n\t"
        "v_exp_f32 v126, v126\n\t"
        "v_exp_f32 v127, v127\n\t"
        "v_cvt_pk_bf16_f32 v154, v124, v125\n\t"
        "v_cvt_pk_bf16_f32 v155, v126, v127\n\t"
        "v_mfma_f32_32x32x8_bf16 v[136:151], v[164:165], v[54:55], v[136:151]\n\t"
        "v_exp_f32 v128, v128\n\t"
        "v_exp_f32 v129, v129\n\t"
        "v_exp_f32 v130, v130\n\t"
        "v_exp_f32 v131, v131\n\t"
        "v_cvt_pk_bf16_f32 v156, v128, v129\n\t"
        "v_cvt_pk_bf16_f32 v157, v130, v131\n\t"
        "v_mfma_f32_32x32x16_bf16 v[88:103], v[166:169], v[152:155], v[88:103]\n\t"
        "v_exp_f32 v132, v132\n\t"
        "v_exp_f32 v133, v133\n\t"
        "v_exp_f32 v134, v134\n\t"
        "v_exp_f32 v135, v135\n\t"
        "v_cvt_pk_bf16_f32 v158, v132, v133\n\t"
        "v_cvt_pk_bf16_f32 v159, v134, v135\n\t"
        "s_waitcnt vmcnt(3)\n\t"
        "v_mfma_f32_32x32x16_bf16 v[120:135], v[174:177], v[32:35], 0\n\t"
        "s_add_u32 s60, s60, 5280\n\t"
        "buffer_load_dwordx4 v[160:163], %[vk16], %[srdkv], s60 offen sc1\n\t"
        "v_exp_f32 v136, v136\n\t"
        "v_exp_f32 v137, v137\n\t"
        "v_exp_f32 v138, v138\n\t"
        "v_exp_f32 v139, v139\n\t"
        "v_cvt_pk_bf16_f32 v152, v136, v137\n\t"
        "v_cvt_pk_bf16_f32 v153, v138, v139\n\t"
        "v_mfma_f32_32x32x16_bf16 v[88:103], v[170:173], v[156:159], v[88:103]\n\t"
        "v_exp_f32 v140, v140\n\t"
        "v_exp_f32 v141, v141\n\t"
        "v_exp_f32 v142, v142\n\t"
        "v_exp_f32 v143, v143\n\t"
        "v_cvt_pk_bf16_f32 v154, v140, v141\n\t"
        "v_cvt_pk_bf16_f32 v155, v142, v143\n\t"
        "s_waitcnt vmcnt(3)\n\t"
        "v_mfma_f32_32x32x8_bf16 v[120:135], v[178:179], v[36:37], v[120:135]\n\t"
        "buffer_load_dwordx2 v[164:165], %[vk8], %[srdkv], s60 offen sc1\n\t"
        "v_exp_f32 v144, v144\n\t"
        "v_exp_f32 v145, v145\n\t"
        "v_exp_f32 v146, v146\n\t"
        "v_exp_f32 v147, v147\n\t"
        "v_cvt_pk_bf16_f32 v156, v144, v145\n\t"
        "v_cvt_pk_bf16_f32 v157, v146, v147\n\t"
        "v_mfma_f32_32x32x16_bf16 v[104:119], v[166:169], v[152:155], v[104:119]\n\t"
        "buffer_load_dwordx4 v[166:169], %[vv], %[srdkv], s60 offen offset:1296 sc1\n\t"
        "v_exp_f32 v148, v148\n\t"
        "v_exp_f32 v149, v149\n\t"
        "v_exp_f32 v150, v150\n\t"
        "v_exp_f32 v151, v151\n\t"
        "v_cvt_pk_bf16_f32 v158, v148, v149\n\t"
        "v_cvt_pk_bf16_f32 v159, v150, v151\n\t"
        "v_mfma_f32_32x32x16_bf16 v[136:151], v[174:177], v[38:41], 0\n\t"
        "v_exp_f32 v120, v120\n\t"
        "v_exp_f32 v121, v121\n\t"
        "v_exp_f32 v122, v122\n\t"
        "v_exp_f32 v123, v123\n\t"
        "v_cvt_pk_bf16_f32 v152, v120, v121\n\t"
        "v_cvt_pk_bf16_f32 v153, v122, v123\n\t"
        "v_mfma_f32_32x32x16_bf16 v[104:119], v[170:173], v[156:159], v[104:119]\n\t"
        "v_exp_f32 v124, v124\n\t"
        "v_exp_f32 v125, v125\n\t"
        "v_exp_f32 v126, v126\n\t"
        "v_exp_f32 v127, v127\n\t"
        "v_cvt_pk_bf16_f32 v154, v124, v125\n\t"
        "v_cvt_pk_bf16_f32 v155, v126, v127\n\t"
        "v_mfma_f32_32x32x8_bf16 v[136:151], v[178:179], v[42:43], v[136:151]\n\t"
        "v_exp_f32 v128, v128\n\t"
        "v_exp_f32 v129, v129\n\t"
        "v_exp_f32 v130, v130\n\t"
        "v_exp_f32 v131, v131\n\t"
        "v_cvt_pk_bf16_f32 v156, v128, v129\n\t"
        "v_cvt_pk_bf16_f32 v157, v130, v131\n\t"
        "s_waitcnt vmcnt(4)\n\t"
        "v_mfma_f32_32x32x16_bf16 v[56:71], v[180:183], v[152:155], v[56:71]\n\t"
        "v_exp_f32 v132, v132\n\t"
        "v_exp_f32 v133, v133\n\t"
        "v_exp_f32 v134, v134\n\t"
        "v_exp_f32 v135, v135\n\t"
        "v_cvt_pk_bf16_f32 v158, v132, v133\n\t"
        "v_cvt_pk_bf16_f32 v159, v134, v135\n\t"
        "v_mfma_f32_32x32x16_bf16 v[120:135], v[174:177], v[44:47], 0\n\t"
        "buffer_load_dwordx4 v[170:173], %[vv], %[srdkv], s60 offen offset:1968 sc1\n\t"
        "v_exp_f32 v136, v136\n\t"
        "v_exp_f32 v137, v137\n\t"
        "v_exp_f32 v138, v138\n\t"
        "v_exp_f32 v139, v139\n\t"
        "v_cvt_pk_bf16_f32 v152, v136, v137\n\t"
        "v_cvt_pk_bf16_f32 v153, v138, v139\n\t"
        "s_waitcnt vmcnt(4)\n\t"
        "v_mfma_f32_32x32x16_bf16 v[56:71], v[184:187], v[156:159], v[56:71]\n\t"
        "v_exp_f32 v140, v140\n\t"
        "v_exp_f32 v141, v141\n\t"
        "v_exp_f32 v142, v142\n\t"
        "v_exp_f32 v143, v143\n\t"
        "v_cvt_pk_bf16_f32 v154, v140, v141\n\t"
        "v_cvt_pk_bf16_f32 v155, v142, v143\n\t"
        "v_mfma_f32_32x32x8_bf16 v[120:135], v[178:179], v[48:49], v[120:135]\n\t"
        "v_exp_f32 v144, v144\n\t"
        "v_exp_f32 v145, v145\n\t"
        "v_exp_f32 v146, v146\n\t"
        "v_exp_f32 v147, v147\n\t"
        "v_cvt_pk_bf16_f32 v156, v144, v145\n\t"
        "v_cvt_pk_bf16_f32 v157, v146, v147\n\t"
        "v_mfma_f32_32x32x16_bf16 v[72:87], v[180:183], v[152:155], v[72:87]\n\t"
        "v_exp_f32 v148, v148\n\t"
        "v_exp_f32 v149, v149\n\t"
        "v_exp_f32 v150, v150\n\t"
        "v_exp_f32 v151, v151\n\t"
        "v_cvt_pk_bf16_f32 v158, v148, v149\n\t"
        "v_cvt_pk_bf16_f32 v159, v150, v151\n\t"
        "v_mfma_f32_32x32x16_bf16 v[136:151], v[174:177], v[50:53], 0\n\t"
        "v_exp_f32 v120, v120\n\t"
        "v_exp_f32 v121, v121\n\t"
        "v_exp_f32 v122, v122\n\t"
        "v_exp_f32 v123, v123\n\t"
        "v_cvt_pk_bf16_f32 v152, v120, v121\n\t"
        "v_cvt_pk_bf16_f32 v153, v122, v123\n\t"
        "v_mfma_f32_32x32x16_bf16 v[72:87], v[184:187], v[156:159], v[72:87]\n\t"
        "v_exp_f32 v124, v124\n\t"
        "v_exp_f32 v125, v125\n\t"
        "v_exp_f32 v126, v126\n\t"
        "v_exp_f32 v127, v127\n\t"
        "v_cvt_pk_bf16_f32 v154, v124, v125\n\t"
        "v_cvt_pk_bf16_f32 v155, v126, v127\n\t"
        "v_mfma_f32_32x32x8_bf16 v[136:151], v[178:179], v[54:55], v[136:151]\n\t"
        "v_exp_f32 v128, v128\n\t"
        "v_exp_f32 v129, v129\n\t"
        "v_exp_f32 v130, v130\n\t"
        "v_exp_f32 v131, v131\n\t"
        "v_cvt_pk_bf16_f32 v156, v128, v129\n\t"
        "v_cvt_pk_bf16_f32 v157, v130, v131\n\t"
        "v_mfma_f32_32x32x16_bf16 v[88:103], v[180:183], v[152:155], v[88:103]\n\t"
        "v_exp_f32 v132, v132\n\t"
        "v_exp_f32 v133, v133\n\t"
        "v_exp_f32 v134, v134\n\t"
        "v_exp_f32 v135, v135\n\t"
        "v_cvt_pk_bf16_f32 v158, v132, v133\n\t"
        "v_cvt_pk_bf16_f32 v159, v134, v135\n\t"
        "s_waitcnt vmcnt(3)\n\t"
        "v_mfma_f32_32x32x16_bf16 v[120:135], v[160:163], v[32:35], 0\n\t"
        "s_add_u32 s61, s61, 5280\n\t"
        "buffer_load_dwordx4 v[174:177], %[vk16], %[srdkv], s61 offen sc1\n\t"
        "v_exp_f32 v136, v136\n\t"
        "v_exp_f32 v137, v137\n\t"
        "v_exp_f32 v138, v138\n\t"
        "v_exp_f32 v139, v139\n\t"
        "v_cvt_pk_bf16_f32 v152, v136, v137\n\t"
        "v_cvt_pk_bf16_f32 v153, v138, v139\n\t"
        "v_mfma_f32_32x32x16_bf16 v[88:103], v[184:187], v[156:159], v[88:103]\n\t"
        "v_exp_f32 v140, v140\n\t"
        "v_exp_f32 v141, v141\n\t"
        "v_exp_f32 v142, v142\n\t"
        "v_exp_f32 v143, v143\n\t"
        "v_cvt_pk_bf16_f32 v154, v140, v141\n\t"
        "v_cvt_pk_bf16_f32 v155, v142, v143\n\t"
        "s_waitcnt vmcnt(3)\n\t"
        "v_mfma_f32_32x32x8_bf16 v[120:135], v[164:165], v[36:37], v[120:135]\n\t"
        "buffer_load_dwordx2 v[178:179], %[vk8], %[srdkv], s61 offen sc1\n\t"
        "v_exp_f32 v144, v144\n\t"
        "v_exp_f32 v145, v145\n\t"
        "v_exp_f32 v146, v146\n\t"
        "v_exp_f32 v147, v147\n\t"
        "v_cvt_pk_bf16_f32 v156, v144, v145\n\t"
        "v_cvt_pk_bf16_f32 v157, v146, v147\n\t"
        "v_mfma_f32_32x32x16_bf16 v[104:119], v[180:183], v[152:155], v[104:119]\n\t"
        "buffer_load_dwordx4 v[180:183], %[vv], %[srdkv], s61 offen offset:1296 sc1\n\t"
        "v_exp_f32 v148, v148\n\t"
        "v_exp_f32 v149, v149\n\t"
        "v_exp_f32 v150, v150\n\t"
        "v_exp_f32 v151, v151\n\t"
        "v_cvt_pk_bf16_f32 v158, v148, v149\n\t"
        "v_cvt_pk_bf16_f32 v159, v150, v151\n\t"
        "v_mfma_f32_32x32x16_bf16 v[136:151], v[160:163], v[38:41], 0\n\t"
        "v_exp_f32 v120, v120\n\t"
        "v_exp_f32 v121, v121\n\t"
        "v_exp_f32 v122, v122\n\t"
        "v_exp_f32 v123, v123\n\t"
        "v_cvt_pk_bf16_f32 v152, v120, v121\n\t"
        "v_cvt_pk_bf16_f32 v153, v122, v123\n\t"
        "v_mfma_f32_32x32x16_bf16 v[104:119], v[184:187], v[156:159], v[104:119]\n\t"
        "v_exp_f32 v124, v124\n\t"
        "v_exp_f32 v125, v125\n\t"
        "v_exp_f32 v126, v126\n\t"
        "v_exp_f32 v127, v127\n\t"
        "v_cvt_pk_bf16_f32 v154, v124, v125\n\t"
        "v_cvt_pk_bf16_f32 v155, v126, v127\n\t"
        "v_mfma_f32_32x32x8_bf16 v[136:151], v[164:165], v[42:43], v[136:151]\n\t"
        "v_exp_f32 v128, v128\n\t"
        "v_exp_f32 v129, v129\n\t"
        "v_exp_f32 v130, v130\n\t"
        "v_exp_f32 v131, v131\n\t"
        "v_cvt_pk_bf16_f32 v156, v128, v129\n\t"
        "v_cvt_pk_bf16_f32 v157, v130, v131\n\t"
        "s_waitcnt vmcnt(4)\n\t"
        "v_mfma_f32_32x32x16_bf16 v[56:71], v[166:169], v[152:155], v[56:71]\n\t"
        "v_exp_f32 v132, v132\n\t"
        "v_exp_f32 v133, v133\n\t"
        "v_exp_f32 v134, v134\n\t"
        "v_exp_f32 v135, v135\n\t"
        "v_cvt_pk_bf16_f32 v158, v132, v133\n\t"
        "v_cvt_pk_bf16_f32 v159, v134, v135\n\t"
        "v_mfma_f32_32x32x16_bf16 v[120:135], v[160:163], v[44:47], 0\n\t"
        "buffer_load_dwordx4 v[184:187], %[vv], %[srdkv], s61 offen offset:1968 sc1\n\t"
        "v_exp_f32 v136, v136\n\t"
        "v_exp_f32 v137, v137\n\t"
        "v_exp_f32 v138, v138\n\t"
        "v_exp_f32 v139, v139\n\t"
        "v_cvt_pk_bf16_f32 v152, v136, v137\n\t"
        "v_cvt_pk_bf16_f32 v153, v138, v139\n\t"
        "s_waitcnt vmcnt(4)\n\t"
        "v_mfma_f32_32x32x16_bf16 v[56:71], v[170:173], v[156:159], v[56:71]\n\t"
        "v_exp_f32 v140, v140\n\t"
        "v_exp_f32 v141, v141\n\t"
        "v_exp_f32 v142, v142\n\t"
        "v_exp_f32 v143, v143\n\t"
        "v_cvt_pk_bf16_f32 v154, v140, v141\n\t"
        "v_cvt_pk_bf16_f32 v155, v142, v143\n\t"
        "v_mfma_f32_32x32x8_bf16 v[120:135], v[164:165], v[48:49], v[120:135]\n\t"
        "v_exp_f32 v144, v144\n\t"
        "v_exp_f32 v145, v145\n\t"
        "v_exp_f32 v146, v146\n\t"
        "v_exp_f32 v147, v147\n\t"
        "v_cvt_pk_bf16_f32 v156, v144, v145\n\t"
        "v_cvt_pk_bf16_f32 v157, v146, v147\n\t"
        "v_mfma_f32_32x32x16_bf16 v[72:87], v[166:169], v[152:155], v[72:87]\n\t"
        "v_exp_f32 v148, v148\n\t"
        "v_exp_f32 v149, v149\n\t"
        "v_exp_f32 v150, v150\n\t"
        "v_exp_f32 v151, v151\n\t"
        "v_cvt_pk_bf16_f32 v158, v148, v149\n\t"
        "v_cvt_pk_bf16_f32 v159, v150, v151\n\t"
        "v_mfma_f32_32x32x16_bf16 v[136:151], v[160:163], v[50:53], 0\n\t"
        "v_exp_f32 v120, v120\n\t"
        "v_exp_f32 v121, v121\n\t"
        "v_exp_f32 v122, v122\n\t"
        "v_exp_f32 v123, v123\n\t"
        "v_cvt_pk_bf16_f32 v152, v120, v121\n\t"
        "v_cvt_pk_bf16_f32 v153, v122, v123\n\t"
        "v_mfma_f32_32x32x16_bf16 v[72:87], v[170:173], v[156:159], v[72:87]\n\t"
        "v_exp_f32 v124, v124\n\t"
        "v_exp_f32 v125, v125\n\t"
        "v_exp_f32 v126, v126\n\t"
        "v_exp_f32 v127, v127\n\t"
        "v_cvt_pk_bf16_f32 v154, v124, v125\n\t"
        "v_cvt_pk_bf16_f32 v155, v126, v127\n\t"
        "v_mfma_f32_32x32x8_bf16 v[136:151], v[164:165], v[54:55], v[136:151]\n\t"
        "v_exp_f32 v128, v128\n\t"
        "v_exp_f32 v129, v129\n\t"
        "v_exp_f32 v130, v130\n\t"
        "v_exp_f32 v131, v131\n\t"
        "v_cvt_pk_bf16_f32 v156, v128, v129\n\t"
        "v_cvt_pk_bf16_f32 v157, v130, v131\n\t"
        "v_mfma_f32_32x32x16_bf16 v[88:103], v[166:169], v[152:155], v[88:103]\n\t"
        "v_exp_f32 v132, v132\n\t"
        "v_exp_f32 v133, v133\n\t"
        "v_exp_f32 v134, v134\n\t"
        "v_exp_f32 v135, v135\n\t"
        "v_cvt_pk_bf16_f32 v158, v132, v133\n\t"
        "v_cvt_pk_bf16_f32 v159, v134, v135\n\t"
        "s_waitcnt vmcnt(3)\n\t"
        "v_mfma_f32_32x32x16_bf16 v[120:135], v[174:177], v[32:35], 0\n\t"
        "s_add_u32 s60, s60, 5280\n\t"
        "buffer_load_dwordx4 v[160:163], %[vk16], %[srdkv], s60 offen sc1\n\t"
        "v_exp_f32 v136, v136\n\t"
        "v_exp_f32 v137, v137\n\t"
        "v_exp_f32 v138, v138\n\t"
        "v_exp_f32 v139, v139\n\t"
        "v_cvt_pk_bf16_f32 v152, v136, v137\n\t"
        "v_cvt_pk_bf16_f32 v153, v138, v139\n\t"
        "v_mfma_f32_32x32x16_bf16 v[88:103], v[170:173], v[156:159], v[88:103]\n\t"
        "v_exp_f32 v140, v140\n\t"
        "v_exp_f32 v141, v141\n\t"
        "v_exp_f32 v142, v142\n\t"
        "v_exp_f32 v143, v143\n\t"
        "v_cvt_pk_bf16_f32 v154, v140, v141\n\t"
        "v_cvt_pk_bf16_f32 v155, v142, v143\n\t"
        "s_waitcnt vmcnt(3)\n\t"
        "v_mfma_f32_32x32x8_bf16 v[120:135], v[178:179], v[36:37], v[120:135]\n\t"
        "buffer_load_dwordx2 v[164:165], %[vk8], %[srdkv], s60 offen sc1\n\t"
        "v_exp_f32 v144, v144\n\t"
        "v_exp_f32 v145, v145\n\t"
        "v_exp_f32 v146, v146\n\t"
        "v_exp_f32 v147, v147\n\t"
        "v_cvt_pk_bf16_f32 v156, v144, v145\n\t"
        "v_cvt_pk_bf16_f32 v157, v146, v147\n\t"
        "v_mfma_f32_32x32x16_bf16 v[104:119], v[166:169], v[152:155], v[104:119]\n\t"
        "buffer_load_dwordx4 v[166:169], %[vv], %[srdkv], s60 offen offset:1296 sc1\n\t"
        "v_exp_f32 v148, v148\n\t"
        "v_exp_f32 v149, v149\n\t"
        "v_exp_f32 v150, v150\n\t"
        "v_exp_f32 v151, v151\n\t"
        "v_cvt_pk_bf16_f32 v158, v148, v149\n\t"
        "v_cvt_pk_bf16_f32 v159, v150, v151\n\t"
        "v_mfma_f32_32x32x16_bf16 v[136:151], v[174:177], v[38:41], 0\n\t"
        "v_exp_f32 v120, v120\n\t"
        "v_exp_f32 v121, v121\n\t"
        "v_exp_f32 v122, v122\n\t"
        "v_exp_f32 v123, v123\n\t"
        "v_cvt_pk_bf16_f32 v152, v120, v121\n\t"
        "v_cvt_pk_bf16_f32 v153, v122, v123\n\t"
        "v_mfma_f32_32x32x16_bf16 v[104:119], v[170:173], v[156:159], v[104:119]\n\t"
        "v_exp_f32 v124, v124\n\t"
        "v_exp_f32 v125, v125\n\t"
        "v_exp_f32 v126, v126\n\t"
        "v_exp_f32 v127, v127\n\t"
        "v_cvt_pk_bf16_f32 v154, v124, v125\n\t"
        "v_cvt_pk_bf16_f32 v155, v126, v127\n\t"
        "v_mfma_f32_32x32x8_bf16 v[136:151], v[178:179], v[42:43], v[136:151]\n\t"
        "v_exp_f32 v128, v128\n\t"
        "v_exp_f32 v129, v129\n\t"
        "v_exp_f32 v130, v130\n\t"
        "v_exp_f32 v131, v131\n\t"
        "v_cvt_pk_bf16_f32 v156, v128, v129\n\t"
        "v_cvt_pk_bf16_f32 v157, v130, v131\n\t"
        "s_waitcnt vmcnt(4)\n\t"
        "v_mfma_f32_32x32x16_bf16 v[56:71], v[180:183], v[152:155], v[56:71]\n\t"
        "v_exp_f32 v132, v132\n\t"
        "v_exp_f32 v133, v133\n\t"
        "v_exp_f32 v134, v134\n\t"
        "v_exp_f32 v135, v135\n\t"
        "v_cvt_pk_bf16_f32 v158, v132, v133\n\t"
        "v_cvt_pk_bf16_f32 v159, v134, v135\n\t"
        "v_mfma_f32_32x32x16_bf16 v[120:135], v[174:177], v[44:47], 0\n\t"
        "buffer_load_dwordx4 v[170:173], %[vv], %[srdkv], s60 offen offset:1968 sc1\n\t"
        "v_exp_f32 v136, v136\n\t"
        "v_exp_f32 v137, v137\n\t"
        "v_exp_f32 v138, v138\n\t"
        "v_exp_f32 v139, v139\n\t"
        "v_cvt_pk_bf16_f32 v152, v136, v137\n\t"
        "v_cvt_pk_bf16_f32 v153, v138, v139\n\t"
        "s_waitcnt vmcnt(4)\n\t"
        "v_mfma_f32_32x32x16_bf16 v[56:71], v[184:187], v[156:159], v[56:71]\n\t"
        "v_exp_f32 v140, v140\n\t"
        "v_exp_f32 v141, v141\n\t"
        "v_exp_f32 v142, v142\n\t"
        "v_exp_f32 v143, v143\n\t"
        "v_cvt_pk_bf16_f32 v154, v140, v141\n\t"
        "v_cvt_pk_bf16_f32 v155, v142, v143\n\t"
        "v_mfma_f32_32x32x8_bf16 v[120:135], v[178:179], v[48:49], v[120:135]\n\t"
        "v_exp_f32 v144, v144\n\t"
        "v_exp_f32 v145, v145\n\t"
        "v_exp_f32 v146, v146\n\t"
        "v_exp_f32 v147, v147\n\t"
        "v_cvt_pk_bf16_f32 v156, v144, v145\n\t"
        "v_cvt_pk_bf16_f32 v157, v146, v147\n\t"
        "v_mfma_f32_32x32x16_bf16 v[72:87], v[180:183], v[152:155], v[72:87]\n\t"
        "v_exp_f32 v148, v148\n\t"
        "v_exp_f32 v149, v149\n\t"
        "v_exp_f32 v150, v150\n\t"
        "v_exp_f32 v151, v151\n\t"
        "v_cvt_pk_bf16_f32 v158, v148, v149\n\t"
        "v_cvt_pk_bf16_f32 v159, v150, v151\n\t"
        "v_mfma_f32_32x32x16_bf16 v[136:151], v[174:177], v[50:53], 0\n\t"
        "v_exp_f32 v120, v120\n\t"
        "v_exp_f32 v121, v121\n\t"
        "v_exp_f32 v122, v122\n\t"
        "v_exp_f32 v123, v123\n\t"
        "v_cvt_pk_bf16_f32 v152, v120, v121\n\t"
        "v_cvt_pk_bf16_f32 v153, v122, v123\n\t"
        "v_mfma_f32_32x32x16_bf16 v[72:87], v[184:187], v[156:159], v[72:87]\n\t"
        "v_exp_f32 v124, v124\n\t"
        "v_exp_f32 v125, v125\n\t"
        "v_exp_f32 v126, v126\n\t"
        "v_exp_f32 v127, v127\n\t"
        "v_cvt_pk_bf16_f32 v154, v124, v125\n\t"
        "v_cvt_pk_bf16_f32 v155, v126, v127\n\t"
        "v_mfma_f32_32x32x8_bf16 v[136:151], v[178:179], v[54:55], v[136:151]\n\t"
        "v_exp_f32 v128, v128\n\t"
        "v_exp_f32 v129, v129\n\t"
        "v_exp_f32 v130, v130\n\t"
        "v_exp_f32 v131, v131\n\t"
        "v_cvt_pk_bf16_f32 v156, v128, v129\n\t"
        "v_cvt_pk_bf16_f32 v157, v130, v131\n\t"
        "v_mfma_f32_32x32x16_bf16 v[88:103], v[180:183], v[152:155], v[88:103]\n\t"
        "v_exp_f32 v132, v132\n\t"
        "v_exp_f32 v133, v133\n\t"
        "v_exp_f32 v134, v134\n\t"
        "v_exp_f32 v135, v135\n\t"
        "v_cvt_pk_bf16_f32 v158, v132, v133\n\t"
        "v_cvt_pk_bf16_f32 v159, v134, v135\n\t"
        "s_waitcnt vmcnt(3)\n\t"
        "v_mfma_f32_32x32x16_bf16 v[120:135], v[160:163], v[32:35], 0\n\t"
        "s_add_u32 s61, s61, 5280\n\t"
        "buffer_load_dwordx4 v[174:177], %[vk16], %[srdkv], s61 offen sc1\n\t"
        "v_exp_f32 v136, v136\n\t"
        "v_exp_f32 v137, v137\n\t"
        "v_exp_f32 v138, v138\n\t"
        "v_exp_f32 v139, v139\n\t"
        "v_cvt_pk_bf16_f32 v152, v136, v137\n\t"
        "v_cvt_pk_bf16_f32 v153, v138, v139\n\t"
        "v_mfma_f32_32x32x16_bf16 v[88:103], v[184:187], v[156:159], v[88:103]\n\t"
        "v_exp_f32 v140, v140\n\t"
        "v_exp_f32 v141, v141\n\t"
        "v_exp_f32 v142, v142\n\t"
        "v_exp_f32 v143, v143\n\t"
        "v_cvt_pk_bf16_f32 v154, v140, v141\n\t"
        "v_cvt_pk_bf16_f32 v155, v142, v143\n\t"
        "s_waitcnt vmcnt(3)\n\t"
        "v_mfma_f32_32x32x8_bf16 v[120:135], v[164:165], v[36:37], v[120:135]\n\t"
        "buffer_load_dwordx2 v[178:179], %[vk8], %[srdkv], s61 offen sc1\n\t"
        "v_exp_f32 v144, v144\n\t"
        "v_exp_f32 v145, v145\n\t"
        "v_exp_f32 v146, v146\n\t"
        "v_exp_f32 v147, v147\n\t"
        "v_cvt_pk_bf16_f32 v156, v144, v145\n\t"
        "v_cvt_pk_bf16_f32 v157, v146, v147\n\t"
        "v_mfma_f32_32x32x16_bf16 v[104:119], v[180:183], v[152:155], v[104:119]\n\t"
        "buffer_load_dwordx4 v[180:183], %[vv], %[srdkv], s61 offen offset:1296 sc1\n\t"
        "v_exp_f32 v148, v148\n\t"
        "v_exp_f32 v149, v149\n\t"
        "v_exp_f32 v150, v150\n\t"
        "v_exp_f32 v151, v151\n\t"
        "v_cvt_pk_bf16_f32 v158, v148, v149\n\t"
        "v_cvt_pk_bf16_f32 v159, v150, v151\n\t"
        "s_setprio 2\n\t"
        "v_mfma_f32_32x32x16_bf16 v[136:151], v[160:163], v[38:41], 0\n\t"
        "v_exp_f32 v120, v120\n\t"
        "v_exp_f32 v121, v121\n\t"
        "v_exp_f32 v122, v122\n\t"
        "v_exp_f32 v123, v123\n\t"
        "v_cvt_pk_bf16_f32 v152, v120, v121\n\t"
        "v_cvt_pk_bf16_f32 v153, v122, v123\n\t"
        "v_mfma_f32_32x32x16_bf16 v[104:119], v[184:187], v[156:159], v[104:119]\n\t"
        "buffer_load_dwordx2 v[232:233], %[vpoll], %[srdf], 0 offen sc1\n\t"
        "buffer_load_dwordx2 v[234:235], %[vpoll], %[srdf], s67 offen sc1\n\t"
        "buffer_load_dwordx2 v[236:237], %[vpoll], %[srdf], s68 offen sc1\n\t"
        "buffer_load_dwordx2 v[238:239], %[vpoll], %[srdf], s69 offen sc1\n\t"
        "v_exp_f32 v124, v124\n\t"
        "v_exp_f32 v125, v125\n\t"
        "v_exp_f32 v126, v126\n\t"
        "v_exp_f32 v127, v127\n\t"
        "v_cvt_pk_bf16_f32 v154, v124, v125\n\t"
        "v_cvt_pk_bf16_f32 v155, v126, v127\n\t"
        "v_mfma_f32_32x32x8_bf16 v[136:151], v[164:165], v[42:43], v[136:151]\n\t"
        "v_exp_f32 v128, v128\n\t"
        "v_exp_f32 v129, v129\n\t"
        "v_exp_f32 v130, v130\n\t"
        "v_exp_f32 v131, v131\n\t"
        "v_cvt_pk_bf16_f32 v156, v128, v129\n\t"
        "v_cvt_pk_bf16_f32 v157, v130, v131\n\t"
        "s_waitcnt vmcnt(8)\n\t"
        "v_mfma_f32_32x32x16_bf16 v[56:71], v[166:169], v[152:155], v[56:71]\n\t"
        "v_exp_f32 v132, v132\n\t"
        "v_exp_f32 v133, v133\n\t"
        "v_exp_f32 v134, v134\n\t"
        "v_exp_f32 v135, v135\n\t"
        "v_cvt_pk_bf16_f32 v158, v132, v133\n\t"
        "v_cvt_pk_bf16_f32 v159, v134, v135\n\t"
        "v_mfma_f32_32x32x16_bf16 v[120:135], v[160:163], v[44:47], 0\n\t"
        "buffer_load_dwordx4 v[184:187], %[vv], %[srdkv], s61 offen offset:1968 sc1\n\t"
        "v_exp_f32 v136, v136\n\t"
        "v_exp_f32 v137, v137\n\t"
        "v_exp_f32 v138, v138\n\t"
        "v_exp_f32 v139, v139\n\t"
        "v_cvt_pk_bf16_f32 v152, v136, v137\n\t"
        "v_cvt_pk_bf16_f32 v153, v138, v139\n\t"
        "s_waitcnt vmcnt(8)\n\t"
        "v_mfma_f32_32x32x16_bf16 v[56:71], v[170:173], v[156:159], v[56:71]\n\t"
        "v_exp_f32 v140, v140\n\t"
        "v_exp_f32 v141, v141\n\t"
        "v_exp_f32 v142, v142\n\t"
        "v_exp_f32 v143, v143\n\t"
        "v_cvt_pk_bf16_f32 v154, v140, v141\n\t"
        "v_cvt_pk_bf16_f32 v155, v142, v143\n\t"
        "v_mfma_f32_32x32x8_bf16 v[120:135], v[164:165], v[48:49], v[120:135]\n\t"
        "v_exp_f32 v144, v144\n\t"
        "v_exp_f32 v145, v145\n\t"
        "v_exp_f32 v146, v146\n\t"
        "v_exp_f32 v147, v147\n\t"
        "v_cvt_pk_bf16_f32 v156, v144, v145\n\t"
        "v_cvt_pk_bf16_f32 v157, v146, v147\n\t"
        "v_mfma_f32_32x32x16_bf16 v[72:87], v[166:169], v[152:155], v[72:87]\n\t"
        "v_exp_f32 v148, v148\n\t"
        "v_exp_f32 v149, v149\n\t"
        "v_exp_f32 v150, v150\n\t"
        "v_exp_f32 v151, v151\n\t"
        "v_cvt_pk_bf16_f32 v158, v148, v149\n\t"
        "v_cvt_pk_bf16_f32 v159, v150, v151\n\t"
        "v_mfma_f32_32x32x16_bf16 v[136:151], v[160:163], v[50:53], 0\n\t"
        "v_exp_f32 v120, v120\n\t"
        "v_exp_f32 v121, v121\n\t"
        "v_exp_f32 v122, v122\n\t"
        "v_exp_f32 v123, v123\n\t"
        "v_cvt_pk_bf16_f32 v152, v120, v121\n\t"
        "v_cvt_pk_bf16_f32 v153, v122, v123\n\t"
        "v_mfma_f32_32x32x16_bf16 v[72:87], v[170:173], v[156:159], v[72:87]\n\t"
        "v_exp_f32 v124, v124\n\t"
        "v_exp_f32 v125, v125\n\t"
        "v_exp_f32 v126, v126\n\t"
        "v_exp_f32 v127, v127\n\t"
        "v_cvt_pk_bf16_f32 v154, v124, v125\n\t"
        "v_cvt_pk_bf16_f32 v155, v126, v127\n\t"
        "v_mfma_f32_32x32x8_bf16 v[136:151], v[164:165], v[54:55], v[136:151]\n\t"
        "v_exp_f32 v128, v128\n\t"
        "v_exp_f32 v129, v129\n\t"
        "v_exp_f32 v130, v130\n\t"
        "v_exp_f32 v131, v131\n\t"
        "v_cvt_pk_bf16_f32 v156, v128, v129\n\t"
        "v_cvt_pk_bf16_f32 v157, v130, v131\n\t"
        "v_mfma_f32_32x32x16_bf16 v[88:103], v[166:169], v[152:155], v[88:103]\n\t"
        "v_exp_f32 v132, v132\n\t"
        "v_exp_f32 v133, v133\n\t"
        "v_exp_f32 v134, v134\n\t"
        "v_exp_f32 v135, v135\n\t"
        "v_cvt_pk_bf16_f32 v158, v132, v133\n\t"
        "v_cvt_pk_bf16_f32 v159, v134, v135\n\t"
        "s_waitcnt vmcnt(7)\n\t"
        "v_mfma_f32_32x32x16_bf16 v[120:135], v[174:177], v[32:35], 0\n\t"
        "s_add_u32 s60, s60, 5280\n\t"
        "buffer_load_dwordx4 v[160:163], %[vk16], %[srdkv], s60 offen sc1\n\t"
        "v_exp_f32 v136, v136\n\t"
        "v_exp_f32 v137, v137\n\t"
        "v_exp_f32 v138, v138\n\t"
        "v_exp_f32 v139, v139\n\t"
        "v_cvt_pk_bf16_f32 v152, v136, v137\n\t"
        "v_cvt_pk_bf16_f32 v153, v138, v139\n\t"
        "v_mfma_f32_32x32x16_bf16 v[88:103], v[170:173], v[156:159], v[88:103]\n\t"
        "v_exp_f32 v140, v140\n\t"
        "v_exp_f32 v141, v141\n\t"
        "v_exp_f32 v142, v142\n\t"
        "v_exp_f32 v143, v143\n\t"
        "v_cvt_pk_bf16_f32 v154, v140, v141\n\t"
        "v_cvt_pk_bf16_f32 v155, v142, v143\n\t"
        "s_waitcnt vmcnt(7)\n\t"
        "v_mfma_f32_32x32x8_bf16 v[120:135], v[178:179], v[36:37], v[120:135]\n\t"
        "buffer_load_dwordx2 v[164:165], %[vk8], %[srdkv], s60 offen sc1\n\t"
        "v_exp_f32 v144, v144\n\t"
        "v_exp_f32 v145, v145\n\t"
        "v_exp_f32 v146, v146\n\t"
        "v_exp_f32 v147, v147\n\t"
        "v_cvt_pk_bf16_f32 v156, v144, v145\n\t"
        "v_cvt_pk_bf16_f32 v157, v146, v147\n\t"
        "v_mfma_f32_32x32x16_bf16 v[104:119], v[166:169], v[152:155], v[104:119]\n\t"
        "buffer_load_dwordx4 v[166:169], %[vv], %[srdkv], s60 offen offset:1296 sc1\n\t"
        "v_exp_f32 v148, v148\n\t"
        "v_exp_f32 v149, v149\n\t"
        "v_exp_f32 v150, v150\n\t"
        "v_exp_f32 v151, v151\n\t"
        "v_cvt_pk_bf16_f32 v158, v148, v149\n\t"
        "v_cvt_pk_bf16_f32 v159, v150, v151\n\t"
        "v_mfma_f32_32x32x16_bf16 v[136:151], v[174:177], v[38:41], 0\n\t"
        "v_exp_f32 v120, v120\n\t"
        "v_exp_f32 v121, v121\n\t"
        "v_exp_f32 v122, v122\n\t"
        "v_exp_f32 v123, v123\n\t"
        "v_cvt_pk_bf16_f32 v152, v120, v121\n\t"
        "v_cvt_pk_bf16_f32 v153, v122, v123\n\t"
        "v_mfma_f32_32x32x16_bf16 v[104:119], v[170:173], v[156:159], v[104:119]\n\t"
        "v_exp_f32 v124, v124\n\t"
        "v_exp_f32 v125, v125\n\t"
        "v_exp_f32 v126, v126\n\t"
        "v_exp_f32 v127, v127\n\t"
        "v_cvt_pk_bf16_f32 v154, v124, v125\n\t"
        "v_cvt_pk_bf16_f32 v155, v126, v127\n\t"
        "v_mfma_f32_32x32x8_bf16 v[136:151], v[178:179], v[42:43], v[136:151]\n\t"
        "v_exp_f32 v128, v128\n\t"
        "v_exp_f32 v129, v129\n\t"
        "v_exp_f32 v130, v130\n\t"
        "v_exp_f32 v131, v131\n\t"
        "v_cvt_pk_bf16_f32 v156, v128, v129\n\t"
        "v_cvt_pk_bf16_f32 v157, v130, v131\n\t"
        "s_waitcnt vmcnt(8)\n\t"
        "v_mfma_f32_32x32x16_bf16 v[56:71], v[180:183], v[152:155], v[56:71]\n\t"
        "v_exp_f32 v132, v132\n\t"
        "v_exp_f32 v133, v133\n\t"
        "v_exp_f32 v134, v134\n\t"
        "v_exp_f32 v135, v135\n\t"
        "v_cvt_pk_bf16_f32 v158, v132, v133\n\t"
        "v_cvt_pk_bf16_f32 v159, v134, v135\n\t"
        "v_mfma_f32_32x32x16_bf16 v[120:135], v[174:177], v[44:47], 0\n\t"
        "buffer_load_dwordx4 v[170:173], %[vv], %[srdkv], s60 offen offset:1968 sc1\n\t"
        "v_exp_f32 v136, v136\n\t"
        "v_exp_f32 v137, v137\n\t"
        "v_exp_f32 v138, v138\n\t"
        "v_exp_f32 v139, v139\n\t"
        "v_cvt_pk_bf16_f32 v152, v136, v137\n\t"
        "v_cvt_pk_bf16_f32 v153, v138, v139\n\t"
        "s_waitcnt vmcnt(4)\n\t"
        "v_mfma_f32_32x32x16_bf16 v[56:71], v[184:187], v[156:159], v[56:71]\n\t"
        "v_exp_f32 v140, v140\n\t"
        "v_exp_f32 v141, v141\n\t"
        "v_exp_f32 v142, v142\n\t"
        "v_exp_f32 v143, v143\n\t"
        "v_cvt_pk_bf16_f32 v154, v140, v141\n\t"
        "v_cvt_pk_bf16_f32 v155, v142, v143\n\t"
        "v_mfma_f32_32x32x8_bf16 v[120:135], v[178:179], v[48:49], v[120:135]\n\t"
        "v_exp_f32 v144, v144\n\t"
        "v_exp_f32 v145, v145\n\t"
        "v_exp_f32 v146, v146\n\t"
        "v_exp_f32 v147, v147\n\t"
        "v_cvt_pk_bf16_f32 v156, v144, v145\n\t"
        "v_cvt_pk_bf16_f32 v157, v146, v147\n\t"
        "v_mfma_f32_32x32x16_bf16 v[72:87], v[180:183], v[152:155], v[72:87]\n\t"
        "v_exp_f32 v148, v148\n\t"
        "v_exp_f32 v149, v149\n\t"
        "v_exp_f32 v150, v150\n\t"
        "v_exp_f32 v151, v151\n\t"
        "v_cvt_pk_bf16_f32 v158, v148, v149\n\t"
        "v_cvt_pk_bf16_f32 v159, v150, v151\n\t"
        "v_mfma_f32_32x32x16_bf16 v[136:151], v[174:177], v[50:53], 0\n\t"
        "v_exp_f32 v120, v120\n\t"
        "v_exp_f32 v121, v121\n\t"
        "v_exp_f32 v122, v122\n\t"
        "v_exp_f32 v123, v123\n\t"
        "v_cvt_pk_bf16_f32 v152, v120, v121\n\t"
        "v_cvt_pk_bf16_f32 v153, v122, v123\n\t"
        "v_mfma_f32_32x32x16_bf16 v[72:87], v[184:187], v[156:159], v[72:87]\n\t"
        "v_exp_f32 v124, v124\n\t"
        "v_exp_f32 v125, v125\n\t"
        "v_exp_f32 v126, v126\n\t"
        "v_exp_f32 v127, v127\n\t"
        "v_cvt_pk_bf16_f32 v154, v124, v125\n\t"
        "v_cvt_pk_bf16_f32 v155, v126, v127\n\t"
        "v_mfma_f32_32x32x8_bf16 v[136:151], v[178:179], v[54:55], v[136:151]\n\t"
        "v_exp_f32 v128, v128\n\t"
        "v_exp_f32 v129, v129\n\t"
        "v_exp_f32 v130, v130\n\t"
        "v_exp_f32 v131, v131\n\t"
        "v_cvt_pk_bf16_f32 v156, v128, v129\n\t"
        "v_cvt_pk_bf16_f32 v157, v130, v131\n\t"
        "v_mfma_f32_32x32x16_bf16 v[88:103], v[180:183], v[152:155], v[88:103]\n\t"
        "v_exp_f32 v132, v132\n\t"
        "v_exp_f32 v133, v133\n\t"
        "v_exp_f32 v134, v134\n\t"
        "v_exp_f32 v135, v135\n\t"
        "v_cvt_pk_bf16_f32 v158, v132, v133\n\t"
        "v_cvt_pk_bf16_f32 v159, v134, v135\n\t"
        "s_waitcnt vmcnt(3)\n\t"
        "v_mfma_f32_32x32x16_bf16 v[120:135], v[160:163], v[32:35], 0\n\t"
        "s_add_u32 s61, s61, 5280\n\t"
        "buffer_load_dwordx4 v[174:177], %[vk16], %[srdkv], s61 offen sc1\n\t"
        "v_exp_f32 v136, v136\n\t"
        "v_exp_f32 v137, v137\n\t"
        "v_exp_f32 v138, v138\n\t"
        "v_exp_f32 v139, v139\n\t"
        "v_cvt_pk_bf16_f32 v152, v136, v137\n\t"
        "v_cvt_pk_bf16_f32 v153, v138, v139\n\t"
        "v_mfma_f32_32x32x16_bf16 v[88:103], v[184:187], v[156:159], v[88:103]\n\t"
        "v_exp_f32 v140, v140\n\t"
        "v_exp_f32 v141, v141\n\t"
        "v_exp_f32 v142, v142\n\t"
        "v_exp_f32 v143, v143\n\t"
        "v_cvt_pk_bf16_f32 v154, v140, v141\n\t"
        "v_cvt_pk_bf16_f32 v155, v142, v143\n\t"
        "s_waitcnt vmcnt(3)\n\t"
        "v_mfma_f32_32x32x8_bf16 v[120:135], v[164:165], v[36:37], v[120:135]\n\t"
        "buffer_load_dwordx2 v[178:179], %[vk8], %[srdkv], s61 offen sc1\n\t"
        "v_exp_f32 v144, v144\n\t"
        "v_exp_f32 v145, v145\n\t"
        "v_exp_f32 v146, v146\n\t"
        "v_exp_f32 v147, v147\n\t"
        "v_cvt_pk_bf16_f32 v156, v144, v145\n\t"
        "v_cvt_pk_bf16_f32 v157, v146, v147\n\t"
        "v_mfma_f32_32x32x16_bf16 v[104:119], v[180:183], v[152:155], v[104:119]\n\t"
        "buffer_load_dwordx4 v[180:183], %[vv], %[srdkv], s61 offen offset:1296 sc1\n\t"
        "v_exp_f32 v148, v148\n\t"
        "v_exp_f32 v149, v149\n\t"
        "v_exp_f32 v150, v150\n\t"
        "v_exp_f32 v151, v151\n\t"
        "v_cvt_pk_bf16_f32 v158, v148, v149\n\t"
        "v_cvt_pk_bf16_f32 v159, v150, v151\n\t"
        "v_mfma_f32_32x32x16_bf16 v[136:151], v[160:163], v[38:41], 0\n\t"
        "v_exp_f32 v120, v120\n\t"
        "v_exp_f32 v121, v121\n\t"
        "v_exp_f32 v122, v122\n\t"
        "v_exp_f32 v123, v123\n\t"
        "v_cvt_pk_bf16_f32 v152, v120, v121\n\t"
        "v_cvt_pk_bf16_f32 v153, v122, v123\n\t"
        "v_mfma_f32_32x32x16_bf16 v[104:119], v[184:187], v[156:159], v[104:119]\n\t"
        "s_cmp_eq_u32 %[spoller], 0\n\t"
        "s_cbranch_scc1 LBP2_%=\n\t"
        "s_mov_b32 s65, 0x10000\n\t"
        "LBP0_%=:\n\t"
        "v_cmp_ne_u64 vcc, s[72:73], v[232:233]\n\t"
        "v_cmp_ne_u64 s[70:71], s[72:73], v[234:235]\n\t"
        "s_nop 0\n\t"
        "s_or_b64 vcc, vcc, s[70:71]\n\t"
        "v_cmp_ne_u64 s[70:71], s[72:73], v[236:237]\n\t"
        "s_nop 0\n\t"
        "s_or_b64 vcc, vcc, s[70:71]\n\t"
        "v_cmp_ne_u64 s[70:71], s[72:73], v[238:239]\n\t"
        "s_nop 0\n\t"
        "s_or_b64 vcc, vcc, s[70:71]\n\t"
        "s_cbranch_vccz LBP2_%=\n\t"
        "s_sub_u32 s65, s65, 1\n\t"
        "s_cmp_eq_u32 s65, 0\n\t"
        "s_cbranch_scc1 LBP2_%=\n\t"
        "s_sleep 2\n\t"
        "buffer_load_dwordx2 v[232:233], %[vpoll], %[srdf], 0 offen sc1\n\t"
        "buffer_load_dwordx2 v[234:235], %[vpoll], %[srdf], s67 offen sc1\n\t"
        "buffer_load_dwordx2 v[236:237], %[vpoll], %[srdf], s68 offen sc1\n\t"
        "buffer_load_dwordx2 v[238:239], %[vpoll], %[srdf], s69 offen sc1\n\t"
        "s_waitcnt vmcnt(0)\n\t"
        "s_branch LBP0_%=\n\t"
        "LBP2_%=:\n\t"
        "s_barrier\n\t"
        "v_exp_f32 v124, v124\n\t"
        "v_exp_f32 v125, v125\n\t"
        "v_exp_f32 v126, v126\n\t"
        "v_exp_f32 v127, v127\n\t"
        "v_cvt_pk_bf16_f32 v154, v124, v125\n\t"
        "v_cvt_pk_bf16_f32 v155, v126, v127\n\t"
        "v_mfma_f32_32x32x8_bf16 v[136:151], v[164:165], v[42:43], v[136:151]\n\t"
        "v_exp_f32 v128, v128\n\t"
        "v_exp_f32 v129, v129\n\t"
        "v_exp_f32 v130, v130\n\t"
        "v_exp_f32 v131, v131\n\t"
        "v_cvt_pk_bf16_f32 v156, v128, v129\n\t"
        "v_cvt_pk_bf16_f32 v157, v130, v131\n\t"
        "s_waitcnt vmcnt(4)\n\t"
        "v_mfma_f32_32x32x16_bf16 v[56:71], v[166:169], v[152:155], v[56:71]\n\t"
        "v_exp_f32 v132, v132\n\t"
        "v_exp_f32 v133, v133\n\t"
        "v_exp_f32 v134, v134\n\t"
        "v_exp_f32 v135, v135\n\t"
        "v_cvt_pk_bf16_f32 v158, v132, v133\n\t"
        "v_cvt_pk_bf16_f32 v159, v134, v135\n\t"
        "v_mfma_f32_32x32x16_bf16 v[120:135], v[160:163], v[44:47], 0\n\t"
        "buffer_load_dwordx4 v[184:187], %[vv], %[srdkv], s61 offen offset:1968 sc1\n\t"
        "v_exp_f32 v136, v136\n\t"
        "v_exp_f32 v137, v137\n\t"
        "v_exp_f32 v138, v138\n\t"
        "v_exp_f32 v139, v139\n\t"
        "v_cvt_pk_bf16_f32 v152, v136, v137\n\t"
        "v_cvt_pk_bf16_f32 v153, v138, v139\n\t"
        "s_waitcnt vmcnt(4)\n\t"
        "v_mfma_f32_32x32x16_bf16 v[56:71], v[170:173], v[156:159], v[56:71]\n\t"
        "s_add_u32 m0, %[sm0], 0\n\t"
        "s_nop 0\n\t"
        "buffer_load_dwordx4 %[vk16], %[srdkv], %[skva] offen sc1 lds\n\t"
        "s_add_u32 m0, %[sm0], 1024\n\t"
        "s_nop 0\n\t"
        "buffer_load_dwordx4 v240, %[srdkv], %[skva] offen sc1 lds\n\t"
        "v_exp_f32 v140, v140\n\t"
        "v_exp_f32 v141, v141\n\t"
        "v_exp_f32 v142, v142\n\t"
        "v_exp_f32 v143, v143\n\t"
        "v_cvt_pk_bf16_f32 v154, v140, v141\n\t"
        "v_cvt_pk_bf16_f32 v155, v142, v143\n\t"
        "v_mfma_f32_32x32x8_bf16 v[120:135], v[164:165], v[48:49], v[120:135]\n\t"
        "v_exp_f32 v144, v144\n\t"
        "v_exp_f32 v145, v145\n\t"
        "v_exp_f32 v146, v146\n\t"
        "v_exp_f32 v147, v147\n\t"
        "v_cvt_pk_bf16_f32 v156, v144, v145\n\t"
        "v_cvt_pk_bf16_f32 v157, v146, v147\n\t"
        "v_mfma_f32_32x32x16_bf16 v[72:87], v[166:169], v[152:155], v[72:87]\n\t"
        "v_exp_f32 v148, v148\n\t"
        "v_exp_f32 v149, v149\n\t"
        "v_exp_f32 v150, v150\n\t"
        "v_exp_f32 v151, v151\n\t"
        "v_cvt_pk_bf16_f32 v158, v148, v149\n\t"
        "v_cvt_pk_bf16_f32 v159, v150, v151\n\t"
        "v_mfma_f32_32x32x16_bf16 v[136:151], v[160:163], v[50:53], 0\n\t"
        "v_exp_f32 v120, v120\n\t"
        "v_exp_f32 v121, v121\n\t"
        "v_exp_f32 v122, v122\n\t"
        "v_exp_f32 v123, v123\n\t"
        "v_cvt_pk_bf16_f32 v152, v120, v121\n\t"
        "v_cvt_pk_bf16_f32 v153, v122, v123\n\t"
        "v_mfma_f32_32x32x16_bf16 v[72:87], v[170:173], v[156:159], v[72:87]\n\t"
        "s_add_u32 m0, %[sm0], 2048\n\t"
        "s_nop 0\n\t"
        "buffer_load_dwordx4 v241, %[srdkv], %[skva] offen sc1 lds\n\t"
        "s_add_u32 m0, %[sm0], 3072\n\t"
        "s_nop 0\n\t"
        "buffer_load_dwordx4 v242, %[srdkv], %[skva] offen sc1 lds\n\t"
        "v_exp_f32 v124, v124\n\t"
        "v_exp_f32 v125, v125\n\t"
        "v_exp_f32 v126, v126\n\t"
        "v_exp_f32 v127, v127\n\t"
        "v_cvt_pk_bf16_f32 v154, v124, v125\n\t"
        "v_cvt_pk_bf16_f32 v155, v126, v127\n\t"
        "v_mfma_f32_32x32x8_bf16 v[136:151], v[164:165], v[54:55], v[136:151]\n\t"
        "v_exp_f32 v128, v128\n\t"
        "v_exp_f32 v129, v129\n\t"
        "v_exp_f32 v130, v130\n\t"
        "v_exp_f32 v131, v131\n\t"
        "v_cvt_pk_bf16_f32 v156, v128, v129\n\t"
        "v_cvt_pk_bf16_f32 v157, v130, v131\n\t"
        "v_mfma_f32_32x32x16_bf16 v[88:103], v[166:169], v[152:155], v[88:103]\n\t"
        "v_exp_f32 v132, v132\n\t"
        "v_exp_f32 v133, v133\n\t"
        "v_exp_f32 v134, v134\n\t"
        "v_exp_f32 v135, v135\n\t"
        "v_cvt_pk_bf16_f32 v158, v132, v133\n\t"
        "v_cvt_pk_bf16_f32 v159, v134, v135\n\t"
        "s_waitcnt vmcnt(7)\n\t"
        "v_mfma_f32_32x32x16_bf16 v[120:135], v[174:177], v[32:35], 0\n\t"
        "s_add_u32 s60, s60, 5280\n\t"
        "buffer_load_dwordx4 v[160:163], %[vk16], %[srdkv], s60 offen sc1\n\t"
        "v_exp_f32 v136, v136\n\t"
        "v_exp_f32 v137, v137\n\t"
        "v_exp_f32 v138, v138\n\t"
        "v_exp_f32 v139, v139\n\t"
        "v_cvt_pk_bf16_f32 v152, v136, v137\n\t"
        "v_cvt_pk_bf16_f32 v153, v138, v139\n\t"
        "v_mfma_f32_32x32x16_bf16 v[88:103], v[170:173], v[156:159], v[88:103]\n\t"
        "v_exp_f32 v140, v140\n\t"
        "v_exp_f32 v141, v141\n\t"
        "v_exp_f32 v142, v142\n\t"
        "v_exp_f32 v143, v143\n\t"
        "v_cvt_pk_bf16_f32 v154, v140, v141\n\t"
        "v_cvt_pk_bf16_f32 v155, v142, v143\n\t"
        "s_waitcnt vmcnt(7)\n\t"
        "v_mfma_f32_32x32x8_bf16 v[120:135], v[178:179], v[36:37], v[120:135]\n\t"
        "buffer_load_dwordx2 v[164:165], %[vk8], %[srdkv], s60 offen sc1\n\t"
        "v_exp_f32 v144, v144\n\t"
        "v_exp_f32 v145, v145\n\t"
        "v_exp_f32 v146, v146\n\t"
        "v_exp_f32 v147, v147\n\t"
        "v_cvt_pk_bf16_f32 v156, v144, v145\n\t"
        "v_cvt_pk_bf16_f32 v157, v146, v147\n\t"
        "v_mfma_f32_32x32x16_bf16 v[104:119], v[166:169], v[152:155], v[104:119]\n\t"
        "buffer_load_dwordx4 v[166:169], %[vv], %[srdkv], s60 offen offset:1296 sc1\n\t"
        "v_exp_f32 v148, v148\n\t"
        "v_exp_f32 v149, v149\n\t"
        "v_exp_f32 v150, v150\n\t"
        "v_exp_f32 v151, v151\n\t"
        "v_cvt_pk_bf16_f32 v158, v148, v149\n\t"
        "v_cvt_pk_bf16_f32 v159, v150, v151\n\t"
        "v_mfma_f32_32x32x16_bf16 v[136:151], v[174:177], v[38:41], 0\n\t"
        "v_exp_f32 v120, v120\n\t"
        "v_exp_f32 v121, v121\n\t"
        "v_exp_f32 v122, v122\n\t"
        "v_exp_f32 v123, v123\n\t"
        "v_cvt_pk_bf16_f32 v152, v120, v121\n\t"
        "v_cvt_pk_bf16_f32 v153, v122, v123\n\t"
        "v_mfma_f32_32x32x16_bf16 v[104:119], v[170:173], v[156:159], v[104:119]\n\t"
        "s_add_u32 m0, %[sm0], 4096\n\t"
        "s_nop 0\n\t"
        "buffer_load_dwordx4 v243, %[srdkv], %[skva] offen sc1 lds\n\t"
        "s_add_u32 s64, %[skva], 3936\n\t"
        "s_add_u32 m0, %[sm0], 5120\n\t"
        "s_nop 0\n\t"
        "buffer_load_dwordx4 %[vv], %[srdkv], s64 offen sc1 lds\n\t"
        "v_exp_f32 v124, v124\n\t"
        "v_exp_f32 v125, v125\n\t"
        "v_exp_f32 v126, v126\n\t"
        "v_exp_f32 v127, v127\n\t"
        "v_cvt_pk_bf16_f32 v154, v124, v125\n\t"
        "v_cvt_pk_bf16_f32 v155, v126, v127\n\t"
        "v_mfma_f32_32x32x8_bf16 v[136:151], v[178:179], v[42:43], v[136:151]\n\t"
        "v_exp_f32 v128, v128\n\t"
        "v_exp_f32 v129, v129\n\t"
        "v_exp_f32 v130, v130\n\t"
        "v_exp_f32 v131, v131\n\t"
        "v_cvt_pk_bf16_f32 v156, v128, v129\n\t"
        "v_cvt_pk_bf16_f32 v157, v130, v131\n\t"
        "s_waitcnt vmcnt(10)\n\t"
        "v_mfma_f32_32x32x16_bf16 v[56:71], v[180:183], v[152:155], v[56:71]\n\t"
        "v_exp_f32 v132, v132\n\t"
        "v_exp_f32 v133, v133\n\t"
        "v_exp_f32 v134, v134\n\t"
        "v_exp_f32 v135, v135\n\t"
        "v_cvt_pk_bf16_f32 v158, v132, v133\n\t"
        "v_cvt_pk_bf16_f32 v159, v134, v135\n\t"
        "v_mfma_f32_32x32x16_bf16 v[120:135], v[174:177], v[44:47], 0\n\t"
        "buffer_load_dwordx4 v[170:173], %[vv], %[srdkv], s60 offen offset:1968 sc1\n\t"
        "v_exp_f32 v136, v136\n\t"
        "v_exp_f32 v137, v137\n\t"
        "v_exp_f32 v138, v138\n\t"
        "v_exp_f32 v139, v139\n\t"
        "v_cvt_pk_bf16_f32 v152, v136, v137\n\t"
        "v_cvt_pk_bf16_f32 v153, v138, v139\n\t"
        "s_waitcnt vmcnt(10)\n\t"
        "v_mfma_f32_32x32x16_bf16 v[56:71], v[184:187], v[156:159], v[56:71]\n\t"
        "s_add_u32 s65, %[skva], 4608\n\t"
        "s_add_u32 m0, %[sm0], 6144\n\t"
        "s_nop 0\n\t"
        "buffer_load_dwordx4 %[vv], %[srdkv], s65 offen sc1 lds\n\t"
        "v_exp_f32 v140, v140\n\t"
        "v_exp_f32 v141, v141\n\t"
        "v_exp_f32 v142, v142\n\t"
        "v_exp_f32 v143, v143\n\t"
        "v_cvt_pk_bf16_f32 v154, v140, v141\n\t"
        "v_cvt_pk_bf16_f32 v155, v142, v143\n\t"
        "v_mfma_f32_32x32x8_bf16 v[120:135], v[178:179], v[48:49], v[120:135]\n\t"
        "v_exp_f32 v144, v144\n\t"
        "v_exp_f32 v145, v145\n\t"
        "v_exp_f32 v146, v146\n\t"
        "v_exp_f32 v147, v147\n\t"
        "v_cvt_pk_bf16_f32 v156, v144, v145\n\t"
        "v_cvt_pk_bf16_f32 v157, v146, v147\n\t"
        "v_mfma_f32_32x32x16_bf16 v[72:87], v[180:183], v[152:155], v[72:87]\n\t"
        "v_exp_f32 v148, v148\n\t"
        "v_exp_f32 v149, v149\n\t"
        "v_exp_f32 v150, v150\n\t"
        "v_exp_f32 v151, v151\n\t"
        "v_cvt_pk_bf16_f32 v158, v148, v149\n\t"
        "v_cvt_pk_bf16_f32 v159, v150, v151\n\t"
        "v_mfma_f32_32x32x16_bf16 v[136:151], v[174:177], v[50:53], 0\n\t"
        "v_exp_f32 v120, v120\n\t"
        "v_exp_f32 v121, v121\n\t"
        "v_exp_f32 v122, v122\n\t"
        "v_exp_f32 v123, v123\n\t"
        "v_cvt_pk_bf16_f32 v152, v120, v121\n\t"
        "v_cvt_pk_bf16_f32 v153, v122, v123\n\t"
        "v_mfma_f32_32x32x16_bf16 v[72:87], v[184:187], v[156:159], v[72:87]\n\t"
        "buffer_load_dwordx4 v[226:229], %[vk16], %[srdq], %[sqa] offen sc1\n\t"
        "buffer_load_dwordx2 v[230:231], %[vq8], %[srdq], %[sqa] offen offset:1024 sc1\n\t"
        "v_exp_f32 v124, v124\n\t"
        "v_exp_f32 v125, v125\n\t"
        "v_exp_f32 v126, v126\n\t"
        "v_exp_f32 v127, v127\n\t"
        "v_cvt_pk_bf16_f32 v154, v124, v125\n\t"
        "v_cvt_pk_bf16_f32 v155, v126, v127\n\t"
        "v_mfma_f32_32x32x8_bf16 v[136:151], v[178:179], v[54:55], v[136:151]\n\t"
        "v_exp_f32 v128, v128\n\t"
        "v_exp_f32 v129, v129\n\t"
        "v_exp_f32 v130, v130\n\t"
        "v_exp_f32 v131, v131\n\t"
        "v_cvt_pk_bf16_f32 v156, v128, v129\n\t"
        "v_cvt_pk_bf16_f32 v157, v130, v131\n\t"
        "v_mfma_f32_32x32x16_bf16 v[88:103], v[180:183], v[152:155], v[88:103]\n\t"
        "v_exp_f32 v132, v132\n\t"
        "v_exp_f32 v133, v133\n\t"
        "v_exp_f32 v134, v134\n\t"
        "v_exp_f32 v135, v135\n\t"
        "v_cvt_pk_bf16_f32 v158, v132, v133\n\t"
        "v_cvt_pk_bf16_f32 v159, v134, v135\n\t"
        "s_waitcnt vmcnt(8)\n\t"
        "v_mfma_f32_32x32x16_bf16 v[120:135], v[160:163], v[32:35], 0\n\t"
        "s_add_u32 s61, s61, 5280\n\t"
        "buffer_load_dwordx4 v[174:177], %[vk16], %[srdkv], s61 offen sc1\n\t"
        "v_exp_f32 v136, v136\n\t"
        "v_exp_f32 v137, v137\n\t"
        "v_exp_f32 v138, v138\n\t"
        "v_exp_f32 v139, v139\n\t"
        "v_cvt_pk_bf16_f32 v152, v136, v137\n\t"
        "v_cvt_pk_bf16_f32 v153, v138, v139\n\t"
        "v_mfma_f32_32x32x16_bf16 v[88:103], v[184:187], v[156:159], v[88:103]\n\t"
        "v_exp_f32 v140, v140\n\t"
        "v_exp_f32 v141, v141\n\t"
        "v_exp_f32 v142, v142\n\t"
        "v_exp_f32 v143, v143\n\t"
        "v_cvt_pk_bf16_f32 v154, v140, v141\n\t"
        "v_cvt_pk_bf16_f32 v155, v142, v143\n\t"
        "s_waitcnt vmcnt(8)\n\t"
        "v_mfma_f32_32x32x8_bf16 v[120:135], v[164:165], v[36:37], v[120:135]\n\t"
        "buffer_load_dwordx2 v[178:179], %[vk8], %[srdkv], s61 offen sc1\n\t"
        "v_exp_f32 v144, v144\n\t"
        "v_exp_f32 v145, v145\n\t"
        "v_exp_f32 v146, v146\n\t"
        "v_exp_f32 v147, v147\n\t"
        "v_cvt_pk_bf16_f32 v156, v144, v145\n\t"
        "v_cvt_pk_bf16_f32 v157, v146, v147\n\t"
        "v_mfma_f32_32x32x16_bf16 v[104:119], v[180:183], v[152:155], v[104:119]\n\t"
        "buffer_load_dwordx4 v[180:183], %[vv], %[srdkv], s61 offen offset:1296 sc1\n\t"
        "v_exp_f32 v148, v148\n\t"
        "v_exp_f32 v149, v149\n\t"
        "v_exp_f32 v150, v150\n\t"
        "v_exp_f32 v151, v151\n\t"
        "v_cvt_pk_bf16_f32 v158, v148, v149\n\t"
        "v_cvt_pk_bf16_f32 v159, v150, v151\n\t"
        "v_mfma_f32_32x32x16_bf16 v[136:151], v[160:163], v[38:41], 0\n\t"
        "v_exp_f32 v120, v120\n\t"
        "v_exp_f32 v121, v121\n\t"
        "v_exp_f32 v122, v122\n\t"
        "v_exp_f32 v123, v123\n\t"
        "v_cvt_pk_bf16_f32 v152, v120, v121\n\t"
        "v_cvt_pk_bf16_f32 v153, v122, v123\n\t"
        "v_mfma_f32_32x32x16_bf16 v[104:119], v[184:187], v[156:159], v[104:119]\n\t"
        "v_exp_f32 v124, v124\n\t"
        "v_exp_f32 v125, v125\n\t"
        "v_exp_f32 v126, v126\n\t"
        "v_exp_f32 v127, v127\n\t"
        "v_cvt_pk_bf16_f32 v154, v124, v125\n\t"
        "v_cvt_pk_bf16_f32 v155, v126, v127\n\t"
        "v_mfma_f32_32x32x8_bf16 v[136:151], v[164:165], v[42:43], v[136:151]\n\t"
        "v_exp_f32 v128, v128\n\t"
        "v_exp_f32 v129, v129\n\t"
        "v_exp_f32 v130, v130\n\t"
        "v_exp_f32 v131, v131\n\t"
        "v_cvt_pk_bf16_f32 v156, v128, v129\n\t"
        "v_cvt_pk_bf16_f32 v157, v130, v131\n\t"
        "s_waitcnt vmcnt(9)\n\t"
        "v_mfma_f32_32x32x16_bf16 v[56:71], v[166:169], v[152:155], v[56:71]\n\t"
        "v_exp_f32 v132, v132\n\t"
        "v_exp_f32 v133, v133\n\t"
        "v_exp_f32 v134, v134\n\t"
        "v_exp_f32 v135, v135\n\t"
        "v_cvt_pk_bf16_f32 v158, v132, v133\n\t"
        "v_cvt_pk_bf16_f32 v159, v134, v135\n\t"
        "v_mfma_f32_32x32x16_bf16 v[120:135], v[160:163], v[44:47], 0\n\t"
        "buffer_load_dwordx4 v[184:187], %[vv], %[srdkv], s61 offen offset:1968 sc1\n\t"
        "v_exp_f32 v136, v136\n\t"
        "v_exp_f32 v137, v137\n\t"
        "v_exp_f32 v138, v138\n\t"
        "v_exp_f32 v139, v139\n\t"
        "v_cvt_pk_bf16_f32 v152, v136, v137\n\t"
        "v_cvt_pk_bf16_f32 v153, v138, v139\n\t"
        "s_waitcnt vmcnt(7)\n\t"
        "v_mfma_f32_32x32x16_bf16 v[56:71], v[170:173], v[156:159], v[56:71]\n\t"
        "v_exp_f32 v140, v140\n\t"
        "v_exp_f32 v141, v141\n\t"
        "v_exp_f32 v142, v142\n\t"
        "v_exp_f32 v143, v143\n\t"
        "v_cvt_pk_bf16_f32 v154, v140, v141\n\t"
        "v_cvt_pk_bf16_f32 v155, v142, v143\n\t"
        "v_mfma_f32_32x32x8_bf16 v[120:135], v[164:165], v[48:49], v[120:135]\n\t"
        "v_exp_f32 v144, v144\n\t"
        "v_exp_f32 v145, v145\n\t"
        "v_exp_f32 v146, v146\n\t"
        "v_exp_f32 v147, v147\n\t"
        "v_cvt_pk_bf16_f32 v156, v144, v145\n\t"
        "v_cvt_pk_bf16_f32 v157, v146, v147\n\t"
        "v_mfma_f32_32x32x16_bf16 v[72:87], v[166:169], v[152:155], v[72:87]\n\t"
        "v_exp_f32 v148, v148\n\t"
        "v_exp_f32 v149, v149\n\t"
        "v_exp_f32 v150, v150\n\t"
        "v_exp_f32 v151, v151\n\t"
        "v_cvt_pk_bf16_f32 v158, v148, v149\n\t"
        "v_cvt_pk_bf16_f32 v159, v150, v151\n\t"
        "v_mfma_f32_32x32x16_bf16 v[136:151], v[160:163], v[50:53], 0\n\t"
        "v_exp_f32 v120, v120\n\t"
        "v_exp_f32 v121, v121\n\t"
        "v_exp_f32 v122, v122\n\t"
        "v_exp_f32 v123, v123\n\t"
        "v_cvt_pk_bf16_f32 v152, v120, v121\n\t"
        "v_cvt_pk_bf16_f32 v153, v122, v123\n\t"
        "v_mfma_f32_32x32x16_bf16 v[72:87], v[170:173], v[156:159], v[72:87]\n\t"
        "v_exp_f32 v124, v124\n\t"
        "v_exp_f32 v125, v125\n\t"
        "v_exp_f32 v126, v126\n\t"
        "v_exp_f32 v127, v127\n\t"
        "v_cvt_pk_bf16_f32 v154, v124, v125\n\t"
        "v_cvt_pk_bf16_f32 v155, v126, v127\n\t"
        "v_mfma_f32_32x32x8_bf16 v[136:151], v[164:165], v[54:55], v[136:151]\n\t"
        "v_exp_f32 v128, v128\n\t"
        "v_exp_f32 v129, v129\n\t"
        "v_exp_f32 v130, v130\n\t"
        "v_exp_f32 v131, v131\n\t"
        "v_cvt_pk_bf16_f32 v156, v128, v129\n\t"
        "v_cvt_pk_bf16_f32 v157, v130, v131\n\t"
        "v_mfma_f32_32x32x16_bf16 v[88:103], v[166:169], v[152:155], v[88:103]\n\t"
        "v_exp_f32 v132, v132\n\t"
        "v_exp_f32 v133, v133\n\t"
        "v_exp_f32 v134, v134\n\t"
        "v_exp_f32 v135, v135\n\t"
        "v_cvt_pk_bf16_f32 v158, v132, v133\n\t"
        "v_cvt_pk_bf16_f32 v159, v134, v135\n\t"
        "s_waitcnt vmcnt(3)\n\t"
        "v_mfma_f32_32x32x16_bf16 v[120:135], v[174:177], v[32:35], 0\n\t"
        "s_add_u32 s60, s60, 5280\n\t"
        "buffer_load_dwordx4 v[160:163], %[vk16], %[srdkv], s60 offen sc1\n\t"
        "v_exp_f32 v136, v136\n\t"
        "v_exp_f32 v137, v137\n\t"
        "v_exp_f32 v138, v138\n\t"
        "v_exp_f32 v139, v139\n\t"
        "v_cvt_pk_bf16_f32 v152, v136, v137\n\t"
        "v_cvt_pk_bf16_f32 v153, v138, v139\n\t"
        "v_mfma_f32_32x32x16_bf16 v[88:103], v[170:173], v[156:159], v[88:103]\n\t"
        "v_exp_f32 v140, v140\n\t"
        "v_exp_f32 v141, v141\n\t"
        "v_exp_f32 v142, v142\n\t"
        "v_exp_f32 v143, v143\n\t"
        "v_cvt_pk_bf16_f32 v154, v140, v141\n\t"
        "v_cvt_pk_bf16_f32 v155, v142, v143\n\t"
        "s_waitcnt vmcnt(3)\n\t"
        "v_mfma_f32_32x32x8_bf16 v[120:135], v[178:179], v[36:37], v[120:135]\n\t"
        "buffer_load_dwordx2 v[164:165], %[vk8], %[srdkv], s60 offen sc1\n\t"
        "v_exp_f32 v144, v144\n\t"
        "v_exp_f32 v145, v145\n\t"
        "v_exp_f32 v146, v146\n\t"
        "v_exp_f32 v147, v147\n\t"
        "v_cvt_pk_bf16_f32 v156, v144, v145\n\t"
        "v_cvt_pk_bf16_f32 v157, v146, v147\n\t"
        "v_mfma_f32_32x32x16_bf16 v[104:119], v[166:169], v[152:155], v[104:119]\n\t"
        "buffer_load_dwordx4 v[166:169], %[vv], %[srdkv], s60 offen offset:1296 sc1\n\t"
        "v_exp_f32 v148, v148\n\t"
        "v_exp_f32 v149, v149\n\t"
        "v_exp_f32 v150, v150\n\t"
        "v_exp_f32 v151, v151\n\t"
        "v_cvt_pk_bf16_f32 v158, v148, v149\n\t"
        "v_cvt_pk_bf16_f32 v159, v150, v151\n\t"
        "s_setprio 1\n\t"
        "v_mfma_f32_32x32x16_bf16 v[136:151], v[174:177], v[38:41], 0\n\t"
        "v_exp_f32 v120, v120\n\t"
        "v_exp_f32 v121, v121\n\t"
        "v_exp_f32 v122, v122\n\t"
        "v_exp_f32 v123, v123\n\t"
        "v_cvt_pk_bf16_f32 v152, v120, v121\n\t"
        "v_cvt_pk_bf16_f32 v153, v122, v123\n\t"
        "v_mfma_f32_32x32x16_bf16 v[104:119], v[170:173], v[156:159], v[104:119]\n\t"
        "v_exp_f32 v124, v124\n\t"
        "v_exp_f32 v125, v125\n\t"
        "v_exp_f32 v126, v126\n\t"
        "v_exp_f32 v127, v127\n\t"
        "v_cvt_pk_bf16_f32 v154, v124, v125\n\t"
        "v_cvt_pk_bf16_f32 v155, v126, v127\n\t"
        "v_mfma_f32_32x32x8_bf16 v[136:151], v[178:179], v[42:43], v[136:151]\n\t"
        "v_exp_f32 v128, v128\n\t"
        "v_exp_f32 v129, v129\n\t"
        "v_exp_f32 v130, v130\n\t"
        "v_exp_f32 v131, v131\n\t"
        "v_cvt_pk_bf16_f32 v156, v128, v129\n\t"
        "v_cvt_pk_bf16_f32 v157, v130, v131\n\t"
        "s_waitcnt vmcnt(4)\n\t"
        "v_mfma_f32_32x32x16_bf16 v[56:71], v[180:183], v[152:155], v[56:71]\n\t"
        "v_exp_f32 v132, v132\n\t"
        "v_exp_f32 v133, v133\n\t"
        "v_exp_f32 v134, v134\n\t"
        "v_exp_f32 v135, v135\n\t"
        "v_cvt_pk_bf16_f32 v158, v132, v133\n\t"
        "v_cvt_pk_bf16_f32 v159, v134, v135\n\t"
        "v_mfma_f32_32x32x16_bf16 v[120:135], v[174:177], v[44:47], 0\n\t"
        "buffer_load_dwordx4 v[170:173], %[vv], %[srdkv], s60 offen offset:1968 sc1\n\t"
        "v_exp_f32 v136, v136\n\t"
        "v_exp_f32 v137, v137\n\t"
        "v_exp_f32 v138, v138\n\t"
        "v_exp_f32 v139, v139\n\t"
        "v_cvt_pk_bf16_f32 v152, v136, v137\n\t"
        "v_cvt_pk_bf16_f32 v153, v138, v139\n\t"
        "s_waitcnt vmcnt(4)\n\t"
        "v_mfma_f32_32x32x16_bf16 v[56:71], v[184:187], v[156:159], v[56:71]\n\t"
        "v_exp_f32 v140, v140\n\t"
        "v_exp_f32 v141, v141\n\t"
        "v_exp_f32 v142, v142\n\t"
        "v_exp_f32 v143, v143\n\t"
        "v_cvt_pk_bf16_f32 v154, v140, v141\n\t"
        "v_cvt_pk_bf16_f32 v155, v142, v143\n\t"
        "v_mfma_f32_32x32x8_bf16 v[120:135], v[178:179], v[48:49], v[120:135]\n\t"
        "v_exp_f32 v144, v144\n\t"
        "v_exp_f32 v145, v145\n\t"
        "v_exp_f32 v146, v146\n\t"
        "v_exp_f32 v147, v147\n\t"
        "v_cvt_pk_bf16_f32 v156, v144, v145\n\t"
        "v_cvt_pk_bf16_f32 v157, v146, v147\n\t"
        "v_mfma_f32_32x32x16_bf16 v[72:87], v[180:183], v[152:155], v[72:87]\n\t"
        "v_exp_f32 v148, v148\n\t"
        "v_exp_f32 v149, v149\n\t"
        "v_exp_f32 v150, v150\n\t"
        "v_exp_f32 v151, v151\n\t"
        "v_cvt_pk_bf16_f32 v158, v148, v149\n\t"
        "v_cvt_pk_bf16_f32 v159, v150, v151\n\t"
        "v_mfma_f32_32x32x16_bf16 v[136:151], v[174:177], v[50:53], 0\n\t"
        "v_exp_f32 v120, v120\n\t"
        "v_exp_f32 v121, v121\n\t"
        "v_exp_f32 v122, v122\n\t"
        "v_exp_f32 v123, v123\n\t"
        "v_cvt_pk_bf16_f32 v152, v120, v121\n\t"
        "v_cvt_pk_bf16_f32 v153, v122, v123\n\t"
        "v_mfma_f32_32x32x16_bf16 v[72:87], v[184:187], v[156:159], v[72:87]\n\t"
        "v_exp_f32 v124, v124\n\t"
        "v_exp_f32 v125, v125\n\t"
        "v_exp_f32 v126, v126\n\t"
        "v_exp_f32 v127, v127\n\t"
        "v_cvt_pk_bf16_f32 v154, v124, v125\n\t"
        "v_cvt_pk_bf16_f32 v155, v126, v127\n\t"
        "v_mfma_f32_32x32x8_bf16 v[136:151], v[178:179], v[54:55], v[136:151]\n\t"
        "v_exp_f32 v128, v128\n\t"
        "v_exp_f32 v129, v129\n\t"
        "v_exp_f32 v130, v130\n\t"
        "v_exp_f32 v131, v131\n\t"
        "v_cvt_pk_bf16_f32 v156, v128, v129\n\t"
        "v_cvt_pk_bf16_f32 v157, v130, v131\n\t"
        "v_mfma_f32_32x32x16_bf16 v[88:103], v[180:183], v[152:155], v[88:103]\n\t"
        "v_exp_f32 v132, v132\n\t"
        "v_exp_f32 v133, v133\n\t"
        "v_exp_f32 v134, v134\n\t"
        "v_exp_f32 v135, v135\n\t"
        "v_cvt_pk_bf16_f32 v158, v132, v133\n\t"
        "v_cvt_pk_bf16_f32 v159, v134, v135\n\t"
        "s_waitcnt vmcnt(3)\n\t"
        "v_mfma_f32_32x32x16_bf16 v[120:135], v[160:163], v[32:35], 0\n\t"
        "s_add_u32 s61, s61, 5280\n\t"
        "buffer_load_dwordx4 v[174:177], %[vk16], %[srdkv], s61 offen sc1\n\t"
        "v_exp_f32 v136, v136\n\t"
        "v_exp_f32 v137, v137\n\t"
        "v_exp_f32 v138, v138\n\t"
        "v_exp_f32 v139, v139\n\t"
        "v_cvt_pk_bf16_f32 v152, v136, v137\n\t"
        "v_cvt_pk_bf16_f32 v153, v138, v139\n\t"
        "v_mfma_f32_32x32x16_bf16 v[88:103], v[184:187], v[156:159], v[88:103]\n\t"
        "v_exp_f32 v140, v140\n\t"
        "v_exp_f32 v141, v141\n\t"
        "v_exp_f32 v142, v142\n\t"
        "v_exp_f32 v143, v143\n\t"
        "v_cvt_pk_bf16_f32 v154, v140, v141\n\t"
        "v_cvt_pk_bf16_f32 v155, v142, v143\n\t"
        "s_waitcnt vmcnt(3)\n\t"
        "v_mfma_f32_32x32x8_bf16 v[120:135], v[164:165], v[36:37], v[120:135]\n\t"
        "buffer_load_dwordx2 v[178:179], %[vk8], %[srdkv], s61 offen sc1\n\t"
        "v_exp_f32 v144, v144\n\t"
        "v_exp_f32 v145, v145\n\t"
        "v_exp_f32 v146, v146\n\t"
        "v_exp_f32 v147, v147\n\t"
        "v_cvt_pk_bf16_f32 v156, v144, v145\n\t"
        "v_cvt_pk_bf16_f32 v157, v146, v147\n\t"
        "v_mfma_f32_32x32x16_bf16 v[104:119], v[180:183], v[152:155], v[104:119]\n\t"
        "buffer_load_dwordx4 v[180:183], %[vv], %[srdkv], s61 offen offset:1296 sc1\n\t"
        "v_exp_f32 v148, v148\n\t"
        "v_exp_f32 v149, v149\n\t"
        "v_exp_f32 v150, v150\n\t"
        "v_exp_f32 v151, v151\n\t"
        "v_cvt_pk_bf16_f32 v158, v148, v149\n\t"
        "v_cvt_pk_bf16_f32 v159, v150, v151\n\t"
        "v_mfma_f32_32x32x16_bf16 v[136:151], v[160:163], v[38:41], 0\n\t"
        "v_exp_f32 v120, v120\n\t"
        "v_exp_f32 v121, v121\n\t"
        "v_exp_f32 v122, v122\n\t"
        "v_exp_f32 v123, v123\n\t"
        "v_cvt_pk_bf16_f32 v152, v120, v121\n\t"
        "v_cvt_pk_bf16_f32 v153, v122, v123\n\t"
        "v_mfma_f32_32x32x16_bf16 v[104:119], v[184:187], v[156:159], v[104:119]\n\t"
        "v_exp_f32 v124, v124\n\t"
        "v_exp_f32 v125, v125\n\t"
        "v_exp_f32 v126, v126\n\t"
        "v_exp_f32 v127, v127\n\t"
        "v_cvt_pk_bf16_f32 v154, v124, v125\n\t"
        "v_cvt_pk_bf16_f32 v155, v126, v127\n\t"
        "v_mfma_f32_32x32x8_bf16 v[136:151], v[164:165], v[42:43], v[136:151]\n\t"
        "v_exp_f32 v128, v128\n\t"
        "v_exp_f32 v129, v129\n\t"
        "v_exp_f32 v130, v130\n\t"
        "v_exp_f32 v131, v131\n\t"
        "v_cvt_pk_bf16_f32 v156, v128, v129\n\t"
        "v_cvt_pk_bf16_f32 v157, v130, v131\n\t"
        "s_waitcnt vmcnt(4)\n\t"
        "v_mfma_f32_32x32x16_bf16 v[56:71], v[166:169], v[152:155], v[56:71]\n\t"
        "v_exp_f32 v132, v132\n\t"
        "v_exp_f32 v133, v133\n\t"
        "v_exp_f32 v134, v134\n\t"
        "v_exp_f32 v135, v135\n\t"
        "v_cvt_pk_bf16_f32 v158, v132, v133\n\t"
        "v_cvt_pk_bf16_f32 v159, v134, v135\n\t"
        "v_mfma_f32_32x32x16_bf16 v[120:135], v[160:163], v[44:47], 0\n\t"
        "buffer_load_dwordx4 v[184:187], %[vv], %[srdkv], s61 offen offset:1968 sc1\n\t"
        "v_exp_f32 v136, v136\n\t"
        "v_exp_f32 v137, v137\n\t"
        "v_exp_f32 v138, v138\n\t"
        "v_exp_f32 v139, v139\n\t"
        "v_cvt_pk_bf16_f32 v152, v136, v137\n\t"
        "v_cvt_pk_bf16_f32 v153, v138, v139\n\t"
        "s_waitcnt vmcnt(4)\n\t"
        "v_mfma_f32_32x32x16_bf16 v[56:71], v[170:173], v[156:159], v[56:71]\n\t"
        "v_exp_f32 v140, v140\n\t"
        "v_exp_f32 v141, v141\n\t"
        "v_exp_f32 v142, v142\n\t"
        "v_exp_f32 v143, v143\n\t"
        "v_cvt_pk_bf16_f32 v154, v140, v141\n\t"
        "v_cvt_pk_bf16_f32 v155, v142, v143\n\t"
        "v_mfma_f32_32x32x8_bf16 v[120:135], v[164:165], v[48:49], v[120:135]\n\t"
        "v_exp_f32 v144, v144\n\t"
        "v_exp_f32 v145, v145\n\t"
        "v_exp_f32 v146, v146\n\t"
        "v_exp_f32 v147, v147\n\t"
        "v_cvt_pk_bf16_f32 v156, v144, v145\n\t"
        "v_cvt_pk_bf16_f32 v157, v146, v147\n\t"
        "v_mfma_f32_32x32x16_bf16 v[72:87], v[166:169], v[152:155], v[72:87]\n\t"
        "v_exp_f32 v148, v148\n\t"
        "v_exp_f32 v149, v149\n\t"
        "v_exp_f32 v150, v150\n\t"
        "v_exp_f32 v151, v151\n\t"
        "v_cvt_pk_bf16_f32 v158, v148, v149\n\t"
        "v_cvt_pk_bf16_f32 v159, v150, v151\n\t"
        "v_mfma_f32_32x32x16_bf16 v[136:151], v[160:163], v[50:53], 0\n\t"
        "v_exp_f32 v120, v120\n\t"
        "v_exp_f32 v121, v121\n\t"
        "v_exp_f32 v122, v122\n\t"
        "v_exp_f32 v123, v123\n\t"
        "v_cvt_pk_bf16_f32 v152, v120, v121\n\t"
        "v_cvt_pk_bf16_f32 v153, v122, v123\n\t"
        "v_mfma_f32_32x32x16_bf16 v[72:87], v[170:173], v[156:159], v[72:87]\n\t"
        "v_exp_f32 v124, v124\n\t"
        "v_exp_f32 v125, v125\n\t"
        "v_exp_f32 v126, v126\n\t"
        "v_exp_f32 v127, v127\n\t"
        "v_cvt_pk_bf16_f32 v154, v124, v125\n\t"
        "v_cvt_pk_bf16_f32 v155, v126, v127\n\t"
        "v_mfma_f32_32x32x8_bf16 v[136:151], v[164:165], v[54:55], v[136:151]\n\t"
        "v_exp_f32 v128, v128\n\t"
        "v_exp_f32 v129, v129\n\t"
        "v_exp_f32 v130, v130\n\t"
        "v_exp_f32 v131, v131\n\t"
        "v_cvt_pk_bf16_f32 v156, v128, v129\n\t"
        "v_cvt_pk_bf16_f32 v157, v130, v131\n\t"
        "v_mfma_f32_32x32x16_bf16 v[88:103], v[166:169], v[152:155], v[88:103]\n\t"
        "v_exp_f32 v132, v132\n\t"
        "v_exp_f32 v133, v133\n\t"
        "v_exp_f32 v134, v134\n\t"
        "v_exp_f32 v135, v135\n\t"
        "v_cvt_pk_bf16_f32 v158, v132, v133\n\t"
        "v_cvt_pk_bf16_f32 v159, v134, v135\n\t"
        "s_waitcnt vmcnt(3)\n\t"
        "v_mfma_f32_32x32x16_bf16 v[120:135], v[174:177], v[32:35], 0\n\t"
        "s_add_u32 s60, s60, 5280\n\t"
        "buffer_load_dwordx4 v[160:163], %[vk16], %[srdkv], s60 offen sc1\n\t"
        "v_exp_f32 v136, v136\n\t"
        "v_exp_f32 v137, v137\n\t"
        "v_exp_f32 v138, v138\n\t"
        "v_exp_f32 v139, v139\n\t"
        "v_cvt_pk_bf16_f32 v152, v136, v137\n\t"
        "v_cvt_pk_bf16_f32 v153, v138, v139\n\t"
        "v_mfma_f32_32x32x16_bf16 v[88:103], v[170:173], v[156:159], v[88:103]\n\t"
        "v_exp_f32 v140, v140\n\t"
        "v_exp_f32 v141, v141\n\t"
        "v_exp_f32 v142, v142\n\t"
        "v_exp_f32 v143, v143\n\t"
        "v_cvt_pk_bf16_f32 v154, v140, v141\n\t"
        "v_cvt_pk_bf16_f32 v155, v142, v143\n\t"
        "s_waitcnt vmcnt(3)\n\t"
        "v_mfma_f32_32x32x8_bf16 v[120:135], v[178:179], v[36:37], v[120:135]\n\t"
        "buffer_load_dwordx2 v[164:165], %[vk8], %[srdkv], s60 offen sc1\n\t"
        "v_exp_f32 v144, v144\n\t"
        "v_exp_f32 v145, v145\n\t"
        "v_exp_f32 v146, v146\n\t"
        "v_exp_f32 v147, v147\n\t"
        "v_cvt_pk_bf16_f32 v156, v144, v145\n\t"
        "v_cvt_pk_bf16_f32 v157, v146, v147\n\t"
        "v_mfma_f32_32x32x16_bf16 v[104:119], v[166:169], v[152:155], v[104:119]\n\t"
        "buffer_load_dwordx4 v[166:169], %[vv], %[srdkv], s60 offen offset:1296 sc1\n\t"
        "v_exp_f32 v148, v148\n\t"
        "v_exp_f32 v149, v149\n\t"
        "v_exp_f32 v150, v150\n\t"
        "v_exp_f32 v151, v151\n\t"
        "v_cvt_pk_bf16_f32 v158, v148, v149\n\t"
        "v_cvt_pk_bf16_f32 v159, v150, v151\n\t"
        "v_mfma_f32_32x32x16_bf16 v[136:151], v[174:177], v[38:41], 0\n\t"
        "v_exp_f32 v120, v120\n\t"
        "v_exp_f32 v121, v121\n\t"
        "v_exp_f32 v122, v122\n\t"
        "v_exp_f32 v123, v123\n\t"
        "v_cvt_pk_bf16_f32 v152, v120, v121\n\t"
        "v_cvt_pk_bf16_f32 v153, v122, v123\n\t"
        "v_mfma_f32_32x32x16_bf16 v[104:119], v[170:173], v[156:159], v[104:119]\n\t"
        "v_exp_f32 v124, v124\n\t"
        "v_exp_f32 v125, v125\n\t"
        "v_exp_f32 v126, v126\n\t"
        "v_exp_f32 v127, v127\n\t"
        "v_cvt_pk_bf16_f32 v154, v124, v125\n\t"
        "v_cvt_pk_bf16_f32 v155, v126, v127\n\t"
        "v_mfma_f32_32x32x8_bf16 v[136:151], v[178:179], v[42:43], v[136:151]\n\t"
        "v_exp_f32 v128, v128\n\t"
        "v_exp_f32 v129, v129\n\t"
        "v_exp_f32 v130, v130\n\t"
        "v_exp_f32 v131, v131\n\t"
        "v_cvt_pk_bf16_f32 v156, v128, v129\n\t"
        "v_cvt_pk_bf16_f32 v157, v130, v131\n\t"
        "s_waitcnt vmcnt(4)\n\t"
        "v_mfma_f32_32x32x16_bf16 v[56:71], v[180:183], v[152:155], v[56:71]\n\t"
        "v_exp_f32 v132, v132\n\t"
        "v_exp_f32 v133, v133\n\t"
        "v_exp_f32 v134, v134\n\t"
        "v_exp_f32 v135, v135\n\t"
        "v_cvt_pk_bf16_f32 v158, v132, v133\n\t"
        "v_cvt_pk_bf16_f32 v159, v134, v135\n\t"
        "v_mfma_f32_32x32x16_bf16 v[120:135], v[174:177], v[44:47], 0\n\t"
        "buffer_load_dwordx4 v[170:173], %[vv], %[srdkv], s60 offen offset:1968 sc1\n\t"
        "v_exp_f32 v136, v136\n\t"
        "v_exp_f32 v137, v137\n\t"
        "v_exp_f32 v138, v138\n\t"
        "v_exp_f32 v139, v139\n\t"
        "v_cvt_pk_bf16_f32 v152, v136, v137\n\t"
        "v_cvt_pk_bf16_f32 v153, v138, v139\n\t"
        "s_waitcnt vmcnt(4)\n\t"
        "v_mfma_f32_32x32x16_bf16 v[56:71], v[184:187], v[156:159], v[56:71]\n\t"
        "v_exp_f32 v140, v140\n\t"
        "v_exp_f32 v141, v141\n\t"
        "v_exp_f32 v142, v142\n\t"
        "v_exp_f32 v143, v143\n\t"
        "v_cvt_pk_bf16_f32 v154, v140, v141\n\t"
        "v_cvt_pk_bf16_f32 v155, v142, v143\n\t"
        "v_mfma_f32_32x32x8_bf16 v[120:135], v[178:179], v[48:49], v[120:135]\n\t"
        "v_exp_f32 v144, v144\n\t"
        "v_exp_f32 v145, v145\n\t"
        "v_exp_f32 v146, v146\n\t"
        "v_exp_f32 v147, v147\n\t"
        "v_cvt_pk_bf16_f32 v156, v144, v145\n\t"
        "v_cvt_pk_bf16_f32 v157, v146, v147\n\t"
        "v_mfma_f32_32x32x16_bf16 v[72:87], v[180:183], v[152:155], v[72:87]\n\t"
        "v_exp_f32 v148, v148\n\t"
        "v_exp_f32 v149, v149\n\t"
        "v_exp_f32 v150, v150\n\t"
        "v_exp_f32 v151, v151\n\t"
        "v_cvt_pk_bf16_f32 v158, v148, v149\n\t"
        "v_cvt_pk_bf16_f32 v159, v150, v151\n\t"
        "v_mfma_f32_32x32x16_bf16 v[136:151], v[174:177], v[50:53], 0\n\t"
        "v_exp_f32 v120, v120\n\t"
        "v_exp_f32 v121, v121\n\t"
        "v_exp_f32 v122, v122\n\t"
        "v_exp_f32 v123, v123\n\t"
        "v_cvt_pk_bf16_f32 v152, v120, v121\n\t"
        "v_cvt_pk_bf16_f32 v153, v122, v123\n\t"
        "v_mfma_f32_32x32x16_bf16 v[72:87], v[184:187], v[156:159], v[72:87]\n\t"
        "v_exp_f32 v124, v124\n\t"
        "v_exp_f32 v125, v125\n\t"
        "v_exp_f32 v126, v126\n\t"
        "v_exp_f32 v127, v127\n\t"
        "v_cvt_pk_bf16_f32 v154, v124, v125\n\t"
        "v_cvt_pk_bf16_f32 v155, v126, v127\n\t"
        "v_mfma_f32_32x32x8_bf16 v[136:151], v[178:179], v[54:55], v[136:151]\n\t"
        "v_exp_f32 v128, v128\n\t"
        "v_exp_f32 v129, v129\n\t"
        "v_exp_f32 v130, v130\n\t"
        "v_exp_f32 v131, v131\n\t"
        "v_cvt_pk_bf16_f32 v156, v128, v129\n\t"
        "v_cvt_pk_bf16_f32 v157, v130, v131\n\t"
        "v_mfma_f32_32x32x16_bf16 v[88:103], v[180:183], v[152:155], v[88:103]\n\t"
        "v_exp_f32 v132, v132\n\t"
        "v_exp_f32 v133, v133\n\t"
        "v_exp_f32 v134, v134\n\t"
        "v_exp_f32 v135, v135\n\t"
        "v_cvt_pk_bf16_f32 v158, v132, v133\n\t"
        "v_cvt_pk_bf16_f32 v159, v134, v135\n\t"
        "s_waitcnt vmcnt(3)\n\t"
        "v_mfma_f32_32x32x16_bf16 v[120:135], v[160:163], v[32:35], 0\n\t"
        "s_add_u32 s61, s61, 5280\n\t"
        "buffer_load_dwordx4 v[174:177], %[vk16], %[srdkv], s61 offen sc1\n\t"
        "v_exp_f32 v136, v136\n\t"
        "v_exp_f32 v137, v137\n\t"
        "v_exp_f32 v138, v138\n\t"
        "v_exp_f32 v139, v139\n\t"
        "v_cvt_pk_bf16_f32 v152, v136, v137\n\t"
        "v_cvt_pk_bf16_f32 v153, v138, v139\n\t"
        "v_mfma_f32_32x32x16_bf16 v[88:103], v[184:187], v[156:159], v[88:103]\n\t"
        "v_exp_f32 v140, v140\n\t"
        "v_exp_f32 v141, v141\n\t"
        "v_exp_f32 v142, v142\n\t"
        "v_exp_f32 v143, v143\n\t"
        "v_cvt_pk_bf16_f32 v154, v140, v141\n\t"
        "v_cvt_pk_bf16_f32 v155, v142, v143\n\t"
        "s_waitcnt vmcnt(3)\n\t"
        "v_mfma_f32_32x32x8_bf16 v[120:135], v[164:165], v[36:37], v[120:135]\n\t"
        "buffer_load_dwordx2 v[178:179], %[vk8], %[srdkv], s61 offen sc1\n\t"
        "v_exp_f32 v144, v144\n\t"
        "v_exp_f32 v145, v145\n\t"
        "v_exp_f32 v146, v146\n\t"
        "v_exp_f32 v147, v147\n\t"
        "v_cvt_pk_bf16_f32 v156, v144, v145\n\t"
        "v_cvt_pk_bf16_f32 v157, v146, v147\n\t"
        "v_mfma_f32_32x32x16_bf16 v[104:119], v[180:183], v[152:155], v[104:119]\n\t"
        "buffer_load_dwordx4 v[180:183], %[vv], %[srdkv], s61 offen offset:1296 sc1\n\t"
        "v_exp_f32 v148, v148\n\t"
        "v_exp_f32 v149, v149\n\t"
        "v_exp_f32 v150, v150\n\t"
        "v_exp_f32 v151, v151\n\t"
        "v_cvt_pk_bf16_f32 v158, v148, v149\n\t"
        "v_cvt_pk_bf16_f32 v159, v150, v151\n\t"
        "s_setprio 0\n\t"
        "v_mfma_f32_32x32x16_bf16 v[136:151], v[160:163], v[38:41], 0\n\t"
        "v_exp_f32 v120, v120\n\t"
        "v_exp_f32 v121, v121\n\t"
        "v_exp_f32 v122, v122\n\t"
        "v_exp_f32 v123, v123\n\t"
        "v_cvt_pk_bf16_f32 v152, v120, v121\n\t"
        "v_cvt_pk_bf16_f32 v153, v122, v123\n\t"
        "v_mfma_f32_32x32x16_bf16 v[104:119], v[184:187], v[156:159], v[104:119]\n\t"
        "v_exp_f32 v124, v124\n\t"
        "v_exp_f32 v125, v125\n\t"
        "v_exp_f32 v126, v126\n\t"
        "v_exp_f32 v127, v127\n\t"
        "v_cvt_pk_bf16_f32 v154, v124, v125\n\t"
        "v_cvt_pk_bf16_f32 v155, v126, v127\n\t"
        "v_mfma_f32_32x32x8_bf16 v[136:151], v[164:165], v[42:43], v[136:151]\n\t"
        "v_exp_f32 v128, v128\n\t"
        "v_exp_f32 v129, v129\n\t"
        "v_exp_f32 v130, v130\n\t"
        "v_exp_f32 v131, v131\n\t"
        "v_cvt_pk_bf16_f32 v156, v128, v129\n\t"
        "v_cvt_pk_bf16_f32 v157, v130, v131\n\t"
        "s_waitcnt vmcnt(4)\n\t"
        "v_mfma_f32_32x32x16_bf16 v[56:71], v[166:169], v[152:155], v[56:71]\n\t"
        "v_exp_f32 v132, v132\n\t"
        "v_exp_f32 v133, v133\n\t"
        "v_exp_f32 v134, v134\n\t"
        "v_exp_f32 v135, v135\n\t"
        "v_cvt_pk_bf16_f32 v158, v132, v133\n\t"
        "v_cvt_pk_bf16_f32 v159, v134, v135\n\t"
        "v_mfma_f32_32x32x16_bf16 v[120:135], v[160:163], v[44:47], 0\n\t"
        "buffer_load_dwordx4 v[184:187], %[vv], %[srdkv], s61 offen offset:1968 sc1\n\t"
        "v_exp_f32 v136, v136\n\t"
        "v_exp_f32 v137, v137\n\t"
        "v_exp_f32 v138, v138\n\t"
        "v_exp_f32 v139, v139\n\t"
        "v_cvt_pk_bf16_f32 v152, v136, v137\n\t"
        "v_cvt_pk_bf16_f32 v153, v138, v139\n\t"
        "s_waitcnt vmcnt(4)\n\t"
        "v_mfma_f32_32x32x16_bf16 v[56:71], v[170:173], v[156:159], v[56:71]\n\t"
        "v_exp_f32 v140, v140\n\t"
        "v_exp_f32 v141, v141\n\t"
        "v_exp_f32 v142, v142\n\t"
        "v_exp_f32 v143, v143\n\t"
        "v_cvt_pk_bf16_f32 v154, v140, v141\n\t"
        "v_cvt_pk_bf16_f32 v155, v142, v143\n\t"
        "v_mfma_f32_32x32x8_bf16 v[120:135], v[164:165], v[48:49], v[120:135]\n\t"
        "v_exp_f32 v144, v144\n\t"
        "v_exp_f32 v145, v145\n\t"
        "v_exp_f32 v146, v146\n\t"
        "v_exp_f32 v147, v147\n\t"
        "v_cvt_pk_bf16_f32 v156, v144, v145\n\t"
        "v_cvt_pk_bf16_f32 v157, v146, v147\n\t"
        "v_mfma_f32_32x32x16_bf16 v[72:87], v[166:169], v[152:155], v[72:87]\n\t"
        "v_exp_f32 v148, v148\n\t"
        "v_exp_f32 v149, v149\n\t"
        "v_exp_f32 v150, v150\n\t"
        "v_exp_f32 v151, v151\n\t"
        "v_cvt_pk_bf16_f32 v158, v148, v149\n\t"
        "v_cvt_pk_bf16_f32 v159, v150, v151\n\t"
        "v_mfma_f32_32x32x16_bf16 v[136:151], v[160:163], v[50:53], 0\n\t"
        "v_exp_f32 v120, v120\n\t"
        "v_exp_f32 v121, v121\n\t"
        "v_exp_f32 v122, v122\n\t"
        "v_exp_f32 v123, v123\n\t"
        "v_cvt_pk_bf16_f32 v152, v120, v121\n\t"
        "v_cvt_pk_bf16_f32 v153, v122, v123\n\t"
        "v_mfma_f32_32x32x16_bf16 v[72:87], v[170:173], v[156:159], v[72:87]\n\t"
        "v_exp_f32 v124, v124\n\t"
        "v_exp_f32 v125, v125\n\t"
        "v_exp_f32 v126, v126\n\t"
        "v_exp_f32 v127, v127\n\t"
        "v_cvt_pk_bf16_f32 v154, v124, v125\n\t"
        "v_cvt_pk_bf16_f32 v155, v126, v127\n\t"
        "v_mfma_f32_32x32x8_bf16 v[136:151], v[164:165], v[54:55], v[136:151]\n\t"
        "v_exp_f32 v128, v128\n\t"
        "v_exp_f32 v129, v129\n\t"
        "v_exp_f32 v130, v130\n\t"
        "v_exp_f32 v131, v131\n\t"
        "v_cvt_pk_bf16_f32 v156, v128, v129\n\t"
        "v_cvt_pk_bf16_f32 v157, v130, v131\n\t"
        "v_mfma_f32_32x32x16_bf16 v[88:103], v[166:169], v[152:155], v[88:103]\n\t"
        "v_exp_f32 v132, v132\n\t"
        "v_exp_f32 v133, v133\n\t"
        "v_exp_f32 v134, v134\n\t"
        "v_exp_f32 v135, v135\n\t"
        "v_cvt_pk_bf16_f32 v158, v132, v133\n\t"
        "v_cvt_pk_bf16_f32 v159, v134, v135\n\t"
        "s_waitcnt vmcnt(3)\n\t"
        "v_mfma_f32_32x32x16_bf16 v[120:135], v[174:177], v[32:35], 0\n\t"
        "v_exp_f32 v136, v136\n\t"
        "v_exp_f32 v137, v137\n\t"
        "v_exp_f32 v138, v138\n\t"
        "v_exp_f32 v139, v139\n\t"
        "v_cvt_pk_bf16_f32 v152, v136, v137\n\t"
        "v_cvt_pk_bf16_f32 v153, v138, v139\n\t"
        "v_mfma_f32_32x32x16_bf16 v[88:103], v[170:173], v[156:159], v[88:103]\n\t"
        "v_exp_f32 v140, v140\n\t"
        "v_exp_f32 v141, v141\n\t"
        "v_exp_f32 v142, v142\n\t"
        "v_exp_f32 v143, v143\n\t"
        "v_cvt_pk_bf16_f32 v154, v140, v141\n\t"
        "v_cvt_pk_bf16_f32 v155, v142, v143\n\t"
        "s_waitcnt vmcnt(2)\n\t"
        "v_mfma_f32_32x32x8_bf16 v[120:135], v[178:179], v[36:37], v[120:135]\n\t"
        "v_exp_f32 v144, v144\n\t"
        "v_exp_f32 v145, v145\n\t"
        "v_exp_f32 v146, v146\n\t"
        "v_exp_f32 v147, v147\n\t"
        "v_cvt_pk_bf16_f32 v156, v144, v145\n\t"
        "v_cvt_pk_bf16_f32 v157, v146, v147\n\t"
        "v_mfma_f32_32x32x16_bf16 v[104:119], v[166:169], v[152:155], v[104:119]\n\t"
        "v_exp_f32 v148, v148\n\t"
        "v_exp_f32 v149, v149\n\t"
        "v_exp_f32 v150, v150\n\t"
        "v_exp_f32 v151, v151\n\t"
        "v_cvt_pk_bf16_f32 v158, v148, v149\n\t"
        "v_cvt_pk_bf16_f32 v159, v150, v151\n\t"
        "v_mfma_f32_32x32x16_bf16 v[136:151], v[174:177], v[38:41], 0\n\t"
        "v_exp_f32 v120, v120\n\t"
        "v_exp_f32 v121, v121\n\t"
        "v_exp_f32 v122, v122\n\t"
        "v_exp_f32 v123, v123\n\t"
        "v_cvt_pk_bf16_f32 v152, v120, v121\n\t"
        "v_cvt_pk_bf16_f32 v153, v122, v123\n\t"
        "v_mfma_f32_32x32x16_bf16 v[104:119], v[170:173], v[156:159], v[104:119]\n\t"
        "v_exp_f32 v124, v124\n\t"
        "v_exp_f32 v125, v125\n\t"
        "v_exp_f32 v126, v126\n\t"
        "v_exp_f32 v127, v127\n\t"
        "v_cvt_pk_bf16_f32 v154, v124, v125\n\t"
        "v_cvt_pk_bf16_f32 v155, v126, v127\n\t"
        "v_mfma_f32_32x32x8_bf16 v[136:151], v[178:179], v[42:43], v[136:151]\n\t"
        "v_exp_f32 v128, v128\n\t"
        "v_exp_f32 v129, v129\n\t"
        "v_exp_f32 v130, v130\n\t"
        "v_exp_f32 v131, v131\n\t"
        "v_cvt_pk_bf16_f32 v156, v128, v129\n\t"
        "v_cvt_pk_bf16_f32 v157, v130, v131\n\t"
        "s_waitcnt vmcnt(1)\n\t"
        "v_mfma_f32_32x32x16_bf16 v[56:71], v[180:183], v[152:155], v[56:71]\n\t"
        "v_exp_f32 v132, v132\n\t"
        "v_exp_f32 v133, v133\n\t"
        "v_exp_f32 v134, v134\n\t"
        "v_exp_f32 v135, v135\n\t"
        "v_cvt_pk_bf16_f32 v158, v132, v133\n\t"
        "v_cvt_pk_bf16_f32 v159, v134, v135\n\t"
        "v_mfma_f32_32x32x16_bf16 v[120:135], v[174:177], v[44:47], 0\n\t"
        "v_exp_f32 v136, v136\n\t"
        "v_exp_f32 v137, v137\n\t"
        "v_exp_f32 v138, v138\n\t"
        "v_exp_f32 v139, v139\n\t"
        "v_cvt_pk_bf16_f32 v152, v136, v137\n\t"
        "v_cvt_pk_bf16_f32 v153, v138, v139\n\t"
        "s_waitcnt vmcnt(0)\n\t"
        "v_mfma_f32_32x32x16_bf16 v[56:71], v[184:187], v[156:159], v[56:71]\n\t"
        "v_exp_f32 v140, v140\n\t"
        "v_exp_f32 v141, v141\n\t"
        "v_exp_f32 v142, v142\n\t"
        "v_exp_f32 v143, v143\n\t"
        "v_cvt_pk_bf16_f32 v154, v140, v141\n\t"
        "v_cvt_pk_bf16_f32 v155, v142, v143\n\t"
        "v_mfma_f32_32x32x8_bf16 v[120:135], v[178:179], v[48:49], v[120:135]\n\t"
        "v_exp_f32 v144, v144\n\t"
        "v_exp_f32 v145, v145\n\t"
        "v_exp_f32 v146, v146\n\t"
        "v_exp_f32 v147, v147\n\t"
        "v_cvt_pk_bf16_f32 v156, v144, v145\n\t"
        "v_cvt_pk_bf16_f32 v157, v146, v147\n\t"
        "v_mfma_f32_32x32x16_bf16 v[72:87], v[180:183], v[152:155], v[72:87]\n\t"
        "v_exp_f32 v148, v148\n\t"
        "v_exp_f32 v149, v149\n\t"
        "v_exp_f32 v150, v150\n\t"
        "v_exp_f32 v151, v151\n\t"
        "v_cvt_pk_bf16_f32 v158, v148, v149\n\t"
        "v_cvt_pk_bf16_f32 v159, v150, v151\n\t"
        "v_mfma_f32_32x32x16_bf16 v[136:151], v[174:177], v[50:53], 0\n\t"
        "v_exp_f32 v120, v120\n\t"
        "v_exp_f32 v121, v121\n\t"
        "v_exp_f32 v122, v122\n\t"
        "v_exp_f32 v123, v123\n\t"
        "v_cvt_pk_bf16_f32 v152, v120, v121\n\t"
        "v_cvt_pk_bf16_f32 v153, v122, v123\n\t"
        "v_mfma_f32_32x32x16_bf16 v[72:87], v[184:187], v[156:159], v[72:87]\n\t"
        "v_exp_f32 v124, v124\n\t"
        "v_exp_f32 v125, v125\n\t"
        "v_exp_f32 v126, v126\n\t"
        "v_exp_f32 v127, v127\n\t"
        "v_cvt_pk_bf16_f32 v154, v124, v125\n\t"
        "v_cvt_pk_bf16_f32 v155, v126, v127\n\t"
        "v_mfma_f32_32x32x8_bf16 v[136:151], v[178:179], v[54:55], v[136:151]\n\t"
        "v_exp_f32 v128, v128\n\t"
        "v_exp_f32 v129, v129\n\t"
        "v_exp_f32 v130, v130\n\t"
        "v_exp_f32 v131, v131\n\t"
        "v_cvt_pk_bf16_f32 v156, v128, v129\n\t"
        "v_cvt_pk_bf16_f32 v157, v130, v131\n\t"
        "v_mfma_f32_32x32x16_bf16 v[88:103], v[180:183], v[152:155], v[88:103]\n\t"
        "v_exp_f32 v132, v132\n\t"
        "v_exp_f32 v133, v133\n\t"
        "v_exp_f32 v134, v134\n\t"
        "v_exp_f32 v135, v135\n\t"
        "v_cvt_pk_bf16_f32 v158, v132, v133\n\t"
        "v_cvt_pk_bf16_f32 v159, v134, v135\n\t"
        "v_exp_f32 v136, v136\n\t"
        "v_exp_f32 v137, v137\n\t"
        "v_exp_f32 v138, v138\n\t"
        "v_exp_f32 v139, v139\n\t"
        "v_cvt_pk_bf16_f32 v152, v136, v137\n\t"
        "v_cvt_pk_bf16_f32 v153, v138, v139\n\t"
        "v_mfma_f32_32x32x16_bf16 v[88:103], v[184:187], v[156:159], v[88:103]\n\t"
        "v_exp_f32 v140, v140\n\t"
        "v_exp_f32 v141, v141\n\t"
        "v_exp_f32 v142, v142\n\t"
        "v_exp_f32 v143, v143\n\t"
        "v_cvt_pk_bf16_f32 v154, v140, v141\n\t"
        "v_cvt_pk_bf16_f32 v155, v142, v143\n\t"
        "v_exp_f32 v144, v144\n\t"
        "v_exp_f32 v145, v145\n\t"
        "v_exp_f32 v146, v146\n\t"
        "v_exp_f32 v147, v147\n\t"
        "v_cvt_pk_bf16_f32 v156, v144, v145\n\t"
        "v_cvt_pk_bf16_f32 v157, v146, v147\n\t"
        "v_mfma_f32_32x32x16_bf16 v[104:119], v[180:183], v[152:155], v[104:119]\n\t"
        "v_exp_f32 v148, v148\n\t"
        "v_exp_f32 v149, v149\n\t"
        "v_exp_f32 v150, v150\n\t"
        "v_exp_f32 v151, v151\n\t"
        "v_cvt_pk_bf16_f32 v158, v148, v149\n\t"
        "v_cvt_pk_bf16_f32 v159, v150, v151\n\t"
        "s_nop 1\n\t"
        "v_mfma_f32_32x32x16_bf16 v[104:119], v[184:187], v[156:159], v[104:119]\n\t"
        "s_waitcnt vmcnt(0)\n\t"
        "s_nop 15\n\t"
        "v_mov_b32 v84, v189\n\t"
        "v_mov_b32 v100, v190\n\t"
        "v_mov_b32 v116, v191\n\t"
        "s_nop 0\n\t"
        "ds_write_b128 %[vp1], v[72:75] offset:0\n\t"
        "ds_write_b128 %[vp1], v[76:79] offset:1024\n\t"
        "ds_write_b128 %[vp1], v[80:83] offset:2048\n\t"
        "ds_write_b128 %[vp1], v[84:87] offset:3072\n\t"
        "ds_write_b128 %[vp2], v[88:91] offset:0\n\t"
        "ds_write_b128 %[vp2], v[92:95] offset:1024\n\t"
        "ds_write_b128 %[vp2], v[96:99] offset:2048\n\t"
        "ds_write_b128 %[vp2], v[100:103] offset:3072\n\t"
        "ds_write_b128 %[vp3], v[104:107] offset:0\n\t"
        "ds_write_b128 %[vp3], v[108:111] offset:1024\n\t"
        "ds_write_b128 %[vp3], v[112:115] offset:2048\n\t"
        "ds_write_b128 %[vp3], v[116:119] offset:3072\n\t"
        "s_waitcnt lgkmcnt(0)\n\t"
        "s_barrier\n\t"
            : "=&{v[56:71]}"(ob), "=&{v188}"(negm), "=&{v[194:209]}"(cdiag), "=&{v[210:225]}"(cfar), "=&{v[226:229]}"(qa16), "=&{v[230:231]}"(qa8)
            : [vk16] "v"(vk16), [vk8] "v"(vk8), [vq8] "v"(vq8), [vv] "v"(vv), [vp1] "v"(vp1), [vp2] "v"(vp2), [vp3] "v"(vp3),
              [vl31] "v"(l31), [vpub] "v"(vpub), [vpoll] "v"(vpoll),
              [srdq] "s"(srdq), [srdkv] "s"(srdkv), [srdf] "s"(srdf), [sqb] "s"(sqb), [skq] "s"(kq), [skv] "s"(skv),
              [sqa] "s"(sqa), [skva] "s"(skva), [sm0] "s"(sm0), [spoller] "s"(rfl(wid == 4 ? 1u : 0u)), "{s[72:73]}"(tag)
            : "v32", "v33", "v34", "v35", "v36", "v37", "v38", "v39", "v40", "v41", "v42", "v43", "v44", "v45", "v46", "v47", "v48", "v49", "v50", "v51", "v52", "v53", "v54", "v55", "v72", "v73", "v74", "v75", "v76", "v77", "v78", "v79", "v80", "v81", "v82", "v83", "v84", "v85", "v86", "v87", "v88", "v89", "v90", "v91", "v92", "v93", "v94", "v95", "v96", "v97", "v98", "v99", "v100", "v101", "v102", "v103", "v104", "v105", "v106", "v107", "v108", "v109", "v110", "v111", "v112", "v113", "v114", "v115", "v116", "v117", "v118", "v119", "v120", "v121", "v122", "v123", "v124", "v125", "v126", "v127", "v128", "v129", "v130", "v131", "v132", "v133", "v134", "v135", "v136", "v137", "v138", "v139", "v140", "v141", "v142", "v143", "v144", "v145", "v146", "v147", "v148", "v149", "v150", "v151", "v152", "v153", "v154", "v155", "v156", "v157", "v158", "v159", "v160", "v161", "v162", "v163", "v164", "v165", "v166", "v167", "v168", "v169", "v170", "v171", "v172", "v173", "v174", "v175", "v176", "v177", "v178", "v179", "v180", "v181", "v182", "v183", "v184", "v185", "v186", "v187", "v189", "v190", "v191", "v192", "v193", "v232", "v233", "v234", "v235", "v236", "v237", "v238", "v239", "v240", "v241", "v242", "v243", "s60", "s61", "s62", "s63", "s64", "s65", "s67", "s68", "s69", "s70", "s71", "s74", "s75", "s76", "s77", "vcc", "scc", "memory");
    }
    STAMP(4);
    const char* qfA = ws + WS_QF + (size_t)(head * 64 + qb) * QT;
    const char* kvA = ws + WS_KV + (size_t)head * 64 * KVT;
    const rsrc_t ro = mk_rsrc(ws + WS_OF, 2 * QF_BYTES);
    {
        LDSP const char* pp = (LDSP const char*)smem + LDS_PART + wid * 3 * 4096 + lane * 16;
        f32x4 part[3][4];
#pragma unroll
        for (int s = 0; s < 3; ++s)
#pragma unroll
            for (int k = 0; k < 4; ++k) part[s][k] = *(LDSP const f32x4*)(pp + s * 4096 + k * 1024);
        float nm = negm;
#pragma unroll
        for (int s = 0; s < 3; ++s) nm = fminf(nm, part[s][3][0]);
        const float f0 = __builtin_amdgcn_exp2f(nm - negm);
        float o[12];
#pragma unroll
        for (int i = 0; i < 12; ++i) o[i] = ob[i] * f0;
#pragma unroll
        for (int s = 0; s < 3; ++s) { const float f = __builtin_amdgcn_exp2f(nm - part[s][3][0]);
#pragma unroll
            for (int i = 0; i < 12; ++i) o[i] += f * part[s][i >> 2][i & 3]; }
        finish_block(o, qfA + QF_BYTES, kvA + KV_BYTES, 0, 64, 0, qb * 32, ro, QF_BYTES + (qb * 32 + head) * QT);
    }
    asm volatile("s_waitcnt lgkmcnt(0)\n\ts_barrier" ::: "memory");
    const int pbr = wi & 1, pj = xcd, pmt = 2 * pj + ((wi >> 1) & 1), pnt = wi >> 2;
    STAMP(5);
    f32x16 oa;
    {
        const unsigned va16 = ldsb + LDS_AIMG + wid * 3584 + lane * 16, va8 = ldsb + LDS_AIMG + wid * 3584 + lane * 8;
        const unsigned des = qb == 0 ? 8 * 3584 : 7 * 3584;
        const unsigned vd16 = va16 + des, vd8 = va8 + des, qz = rfl(qb == 0 ? 1u : 0u);
        const unsigned vpub = (wid == 4 && lane == 0) ? SY_TB + blockIdx.x * 64 : OOB;
        const u32x4 srdw = make_srd(pbr ? woB : woA, DM * DM * 2);
        const unsigned r6 = lane / 10, vwo = r6 * (DM * 2) + (lane - r6 * 10) * 16;
        asm volatile(
        "s_nop 4\n\t"
        "ds_read_b128 v[174:177], %[vd16]\n\t"
        "ds_read_b64 v[178:179], %[vd8] offset:1024\n\t"
        "ds_read_b128 v[160:163], %[va16] offset:0\n\t"
        "ds_read_b64 v[164:165], %[va8] offset:1024\n\t"
        "ds_read_b128 v[166:169], %[va16] offset:1536\n\t"
        "ds_read_b128 v[170:173], %[va16] offset:2560\n\t"
        "s_mov_b32 s62, 0\n\t"
        "s_mov_b32 s63, -1\n\t"
        "s_waitcnt lgkmcnt(4)\n\t"
        "s_cmp_eq_u32 %[qz], 0\n\t"
        "s_cbranch_scc1 LA1_%=\n\t"
        "v_mfma_f32_32x32x16_bf16 v[136:151], v[174:177], v[226:229], v[194:209]\n\t"
        "s_branch LA2_%=\n\t"
        "LA1_%=:\n\t"
        "v_mfma_f32_32x32x16_bf16 v[136:151], v[174:177], v[226:229], 0\n\t"
        "LA2_%=:\n\t"
        "v_mfma_f32_32x32x8_bf16 v[136:151], v[178:179], v[230:231], v[136:151]\n\t"
        "ds_read_b128 v[174:177], %[va16] offset:3584\n\t"
        "ds_read_b64 v[178:179], %[va8] offset:4608\n\t"
        "s_nop 7\n\t"
        "s_nop 3\n\t"
        "v_max3_f32 v192, v136, v137, v138\n\t"
        "v_max3_f32 v192, v192, v139, v140\n\t"
        "v_max3_f32 v192, v192, v141, v142\n\t"
        "v_max3_f32 v192, v192, v143, v144\n\t"
        "v_max3_f32 v192, v192, v145, v146\n\t"
        "v_max3_f32 v192, v192, v147, v148\n\t"
        "v_max3_f32 v192, v192, v149, v150\n\t"
        "v_max_f32 v192, v192, v151\n\t"
        "v_mov_b32 v193, v192\n\t"
        "s_nop 1\n\t"
        "v_permlane32_swap_b32 v192, v193\n\t"
        "s_nop 1\n\t"
        "v_max_f32 v192, v192, v193\n\t"
        "v_cvt_pk_bf16_f32 v193, -v192, 0\n\t"
        "v_cndmask_b32 v230, v230, v193, s[62:63]\n\t"
        "v_lshlrev_b32 v189, 16, v193\n\t"
        "s_waitcnt lgkmcnt(4)\n\t"
        "s_nop 1\n\t"
        "v_mfma_f32_32x32x16_bf16 v[120:135], v[160:163], v[226:229], v[210:225]\n\t"
        "v_mfma_f32_32x32x8_bf16 v[120:135], v[164:165], v[230:231], v[120:135]\n\t"
        "s_nop 7\n\t"
        "s_nop 3\n\t"
        "s_waitcnt lgkmcnt(1)\n\t"
        "v_mfma_f32_32x32x16_bf16 v[136:151], v[174:177], v[226:229], 0\n\t"
        "ds_read_b128 v[160:163], %[va16] offset:7168\n\t"
        "s_add_u32 s60, %[swid], 0\n\t"
        "s_min_u32 s60, s60, 106\n\t"
        "s_mul_i32 s61, s60, 960\n\t"
        "s_add_u32 m0, s61, %[sdst]\n\t"
        "s_mul_i32 s60, s60, 7680\n\t"
        "s_add_u32 s60, s60, %[scol]\n\t"
        "s_mov_b32 exec_hi, 0x0fffffff\n\t"
        "buffer_load_dwordx4 %[vwo], %[srdw], s60 offen lds\n\t"
        "s_mov_b32 exec_hi, -1\n\t"
        "v_exp_f32 v120, v120\n\t"
        "v_exp_f32 v121, v121\n\t"
        "v_exp_f32 v122, v122\n\t"
        "v_exp_f32 v123, v123\n\t"
        "v_cvt_pk_bf16_f32 v152, v120, v121\n\t"
        "v_cvt_pk_bf16_f32 v153, v122, v123\n\t"
        "ds_read_b128 v[180:183], %[va16] offset:5120\n\t"
        "v_exp_f32 v124, v124\n\t"
        "v_exp_f32 v125, v125\n\t"
        "v_exp_f32 v126, v126\n\t"
        "v_exp_f32 v127, v127\n\t"
        "v_cvt_pk_bf16_f32 v154, v124, v125\n\t"
        "v_cvt_pk_bf16_f32 v155, v126, v127\n\t"
        "s_waitcnt lgkmcnt(2)\n\t"
        "v_mfma_f32_32x32x8_bf16 v[136:151], v[178:179], v[230:231], v[136:151]\n\t"
        "ds_read_b64 v[164:165], %[va8] offset:8192\n\t"
        "s_add_u32 s60, %[swid], 8\n\t"
        "s_min_u32 s60, s60, 106\n\t"
        "s_mul_i32 s61, s60, 960\n\t"
        "s_add_u32 m0, s61, %[sdst]\n\t"
        "s_mul_i32 s60, s60, 7680\n\t"
        "s_add_u32 s60, s60, %[scol]\n\t"
        "s_mov_b32 exec_hi, 0x0fffffff\n\t"
        "buffer_load_dwordx4 %[vwo], %[srdw], s60 offen lds\n\t"
        "s_mov_b32 exec_hi, -1\n\t"
        "v_exp_f32 v128, v128\n\t"
        "v_exp_f32 v129, v129\n\t"
        "v_exp_f32 v130, v130\n\t"
        "v_exp_f32 v131, v131\n\t"
        "v_cvt_pk_bf16_f32 v156, v128, v129\n\t"
        "v_cvt_pk_bf16_f32 v157, v130, v131\n\t"
        "v_mfma_f32_32x32x16_bf16 v[32:47], v[166:169], v[152:155], 0\n\t"
        "ds_read_b128 v[184:187], %[va16] offset:6144\n\t"
        "v_exp_f32 v132, v132\n\t"
        "v_exp_f32 v133, v133\n\t"
        "v_exp_f32 v134, v134\n\t"
        "v_exp_f32 v135, v135\n\t"
        "v_cvt_pk_bf16_f32 v158, v132, v133\n\t"
        "v_cvt_pk_bf16_f32 v159, v134, v135\n\t"
        "s_waitcnt lgkmcnt(3)\n\t"
        "v_mfma_f32_32x32x16_bf16 v[120:135], v[160:163], v[226:229], 0\n\t"
        "ds_read_b128 v[174:177], %[va16] offset:10752\n\t"
        "s_add_u32 s60, %[swid], 16\n\t"
        "s_min_u32 s60, s60, 106\n\t"
        "s_mul_i32 s61, s60, 960\n\t"
        "s_add_u32 m0, s61, %[sdst]\n\t"
        "s_mul_i32 s60, s60, 7680\n\t"
        "s_add_u32 s60, s60, %[scol]\n\t"
        "s_mov_b32 exec_hi, 0x0fffffff\n\t"
        "buffer_load_dwordx4 %[vwo], %[srdw], s60 offen lds\n\t"
        "s_mov_b32 exec_hi, -1\n\t"
        "v_exp_f32 v136, v136\n\t"
        "v_exp_f32 v137, v137\n\t"
        "v_exp_f32 v138, v138\n\t"
        "v_exp_f32 v139, v139\n\t"
        "v_cvt_pk_bf16_f32 v152, v136, v137\n\t"
        "v_cvt_pk_bf16_f32 v153, v138, v139\n\t"
        "v_mfma_f32_32x32x16_bf16 v[32:47], v[170:173], v[156:159], v[32:47]\n\t"
        "ds_read_b128 v[166:169], %[va16] offset:8704\n\t"
        "v_exp_f32 v140, v140\n\t"
        "v_exp_f32 v141, v141\n\t"
        "v_exp_f32 v142, v142\n\t"
        "v_exp_f32 v143, v143\n\t"
        "v_cvt_pk_bf16_f32 v154, v140, v141\n\t"
        "v_cvt_pk_bf16_f32 v155, v142, v143\n\t"
        "s_waitcnt lgkmcnt(3)\n\t"
        "v_mfma_f32_32x32x8_bf16 v[120:135], v[164:165], v[230:231], v[120:135]\n\t"
        "ds_read_b64 v[178:179], %[va8] offset:11776\n\t"
        "s_add_u32 s60, %[swid], 24\n\t"
        "s_min_u32 s60, s60, 106\n\t"
        "s_mul_i32 s61, s60, 960\n\t"
        "s_add_u32 m0, s61, %[sdst]\n\t"
        "s_mul_i32 s60, s60, 7680\n\t"
        "s_add_u32 s60, s60, %[scol]\n\t"
        "s_mov_b32 exec_hi, 0x0fffffff\n\t"
        "buffer_load_dwordx4 %[vwo], %[srdw], s60 offen lds\n\t"
        "s_mov_b32 exec_hi, -1\n\t"
        "v_exp_f32 v144, v144\n\t"
        "v_exp_f32 v145, v145\n\t"
        "v_exp_f32 v146, v146\n\t"
        "v_exp_f32 v147, v147\n\t"
        "v_cvt_pk_bf16_f32 v156, v144, v145\n\t"
        "v_cvt_pk_bf16_f32 v157, v146, v147\n\t"
        "v_mfma_f32_32x32x16_bf16 v[32:47], v[180:183], v[152:155], v[32:47]\n\t"
        "ds_read_b128 v[170:173], %[va16] offset:9728\n\t"
        "v_exp_f32 v148, v148\n\t"
        "v_exp_f32 v149, v149\n\t"
        "v_exp_f32 v150, v150\n\t"
        "v_exp_f32 v151, v151\n\t"
        "v_cvt_pk_bf16_f32 v158, v148, v149\n\t"
        "v_cvt_pk_bf16_f32 v159, v150, v151\n\t"
        "s_waitcnt lgkmcnt(3)\n\t"
        "v_mfma_f32_32x32x16_bf16 v[136:151], v[174:177], v[226:229], 0\n\t"
        "ds_read_b128 v[160:163], %[va16] offset:14336\n\t"
        "s_add_u32 s60, %[swid], 32\n\t"
        "s_min_u32 s60, s60, 106\n\t"
        "s_mul_i32 s61, s60, 960\n\t"
        "s_add_u32 m0, s61, %[sdst]\n\t"
        "s_mul_i32 s60, s60, 7680\n\t"
        "s_add_u32 s60, s60, %[scol]\n\t"
        "s_mov_b32 exec_hi, 0x0fffffff\n\t"
        "buffer_load_dwordx4 %[vwo], %[srdw], s60 offen lds\n\t"
        "s_mov_b32 exec_hi, -1\n\t"
        "v_exp_f32 v120, v120\n\t"
        "v_exp_f32 v121, v121\n\t"
        "v_exp_f32 v122, v122\n\t"
        "v_exp_f32 v123, v123\n\t"
        "v_cvt_pk_bf16_f32 v152, v120, v121\n\t"
        "v_cvt_pk_bf16_f32 v153, v122, v123\n\t"
        "v_mfma_f32_32x32x16_bf16 v[32:47], v[184:187], v[156:159], v[32:47]\n\t"
        "ds_read_b128 v[180:183], %[va16] offset:12288\n\t"
        "v_exp_f32 v124, v124\n\t"
        "v_exp_f32 v125, v125\n\t"
        "v_exp_f32 v126, v126\n\t"
        "v_exp_f32 v127, v127\n\t"
        "v_cvt_pk_bf16_f32 v154, v124, v125\n\t"
        "v_cvt_pk_bf16_f32 v155, v126, v127\n\t"
        "s_waitcnt lgkmcnt(3)\n\t"
        "v_mfma_f32_32x32x8_bf16 v[136:151], v[178:179], v[230:231], v[136:151]\n\t"
        "ds_read_b64 v[164:165], %[va8] offset:15360\n\t"
        "s_add_u32 s60, %[swid], 40\n\t"
        "s_min_u32 s60, s60, 106\n\t"
        "s_mul_i32 s61, s60, 960\n\t"
        "s_add_u32 m0, s61, %[sdst]\n\t"
        "s_mul_i32 s60, s60, 7680\n\t"
        "s_add_u32 s60, s60, %[scol]\n\t"
        "s_mov_b32 exec_hi, 0x0fffffff\n\t"
        "buffer_load_dwordx4 %[vwo], %[srdw], s60 offen lds\n\t"
        "s_mov_b32 exec_hi, -1\n\t"
        "v_exp_f32 v128, v128\n\t"
        "v_exp_f32 v129, v129\n\t"
        "v_exp_f32 v130, v130\n\t"
        "v_exp_f32 v131, v131\n\t"
        "v_cvt_pk_bf16_f32 v156, v128, v129\n\t"
        "v_cvt_pk_bf16_f32 v157, v130, v131\n\t"
        "v_mfma_f32_32x32x16_bf16 v[32:47], v[166:169], v[152:155], v[32:47]\n\t"
        "ds_read_b128 v[184:187], %[va16] offset:13312\n\t"
        "v_exp_f32 v132, v132\n\t"
        "v_exp_f32 v133, v133\n\t"
        "v_exp_f32 v134, v134\n\t"
        "v_exp_f32 v135, v135\n\t"
        "v_cvt_pk_bf16_f32 v158, v132, v133\n\t"
        "v_cvt_pk_bf16_f32 v159, v134, v135\n\t"
        "s_waitcnt lgkmcnt(3)\n\t"
        "v_mfma_f32_32x32x16_bf16 v[120:135], v[160:163], v[226:229], 0\n\t"
        "ds_read_b128 v[174:177], %[va16] offset:17920\n\t"
        "s_add_u32 s60, %[swid], 48\n\t"
        "s_min_u32 s60, s60, 106\n\t"
        "s_mul_i32 s61, s60, 960\n\t"
        "s_add_u32 m0, s61, %[sdst]\n\t"
        "s_mul_i32 s60, s60, 7680\n\t"
        "s_add_u32 s60, s60, %[scol]\n\t"
        "s_mov_b32 exec_hi, 0x0fffffff\n\t"
        "buffer_load_dwordx4 %[vwo], %[srdw], s60 offen lds\n\t"
        "s_mov_b32 exec_hi, -1\n\t"
        "v_exp_f32 v136, v136\n\t"
        "v_exp_f32 v137, v137\n\t"
        "v_exp_f32 v138, v138\n\t"
        "v_exp_f32 v139, v139\n\t"
        "v_cvt_pk_bf16_f32 v152, v136, v137\n\t"
        "v_cvt_pk_bf16_f32 v153, v138, v139\n\t"
        "v_mfma_f32_32x32x16_bf16 v[32:47], v[170:173], v[156:159], v[32:47]\n\t"
        "ds_read_b128 v[166:169], %[va16] offset:15872\n\t"
        "s_waitcnt vmcnt(7)\n\t"
        "s_barrier\n\t"
        "v_mov_b32 v192, s72\n\t"
        "v_mov_b32 v193, s73\n\t"
        "s_nop 0\n\t"
        "buffer_store_dwordx2 v[192:193], %[vpub], %[srdf], 0 offen sc1\n\t"
        "v_exp_f32 v140, v140\n\t"
        "v_exp_f32 v141, v141\n\t"
        "v_exp_f32 v142, v142\n\t"
        "v_exp_f32 v143, v143\n\t"
        "v_cvt_pk_bf16_f32 v154, v140, v141\n\t"
        "v_cvt_pk_bf16_f32 v155, v142, v143\n\t"
        "s_waitcnt lgkmcnt(3)\n\t"
        "v_mfma_f32_32x32x8_bf16 v[120:135], v[164:165], v[230:231], v[120:135]\n\t"
        "ds_read_b64 v[178:179], %[va8] offset:18944\n\t"
        "s_add_u32 s60, %[swid], 56\n\t"
        "s_min_u32 s60, s60, 106\n\t"
        "s_mul_i32 s61, s60, 960\n\t"
        "s_add_u32 m0, s61, %[sdst]\n\t"
        "s_mul_i32 s60, s60, 7680\n\t"
        "s_add_u32 s60, s60, %[scol]\n\t"
        "s_mov_b32 exec_hi, 0x0fffffff\n\t"
        "buffer_load_dwordx4 %[vwo], %[srdw], s60 offen lds\n\t"
        "s_mov_b32 exec_hi, -1\n\t"
        "v_exp_f32 v144, v144\n\t"
        "v_exp_f32 v145, v145\n\t"
        "v_exp_f32 v146, v146\n\t"
        "v_exp_f32 v147, v147\n\t"
        "v_cvt_pk_bf16_f32 v156, v144, v145\n\t"
        "v_cvt_pk_bf16_f32 v157, v146, v147\n\t"
        "v_mfma_f32_32x32x16_bf16 v[32:47], v[180:183], v[152:155], v[32:47]\n\t"
        "ds_read_b128 v[170:173], %[va16] offset:16896\n\t"
        "v_exp_f32 v148, v148\n\t"
        "v_exp_f32 v149, v149\n\t"
        "v_exp_f32 v150, v150\n\t"
        "v_exp_f32 v151, v151\n\t"
        "v_cvt_pk_bf16_f32 v158, v148, v149\n\t"
        "v_cvt_pk_bf16_f32 v159, v150, v151\n\t"
        "s_waitcnt lgkmcnt(3)\n\t"
        "v_mfma_f32_32x32x16_bf16 v[136:151], v[174:177], v[226:229], 0\n\t"
        "ds_read_b128 v[160:163], %[va16] offset:21504\n\t"
        "s_add_u32 s60, %[swid], 64\n\t"
        "s_min_u32 s60, s60, 106\n\t"
        "s_mul_i32 s61, s60, 960\n\t"
        "s_add_u32 m0, s61, %[sdst]\n\t"
        "s_mul_i32 s60, s60, 7680\n\t"
        "s_add_u32 s60, s60, %[scol]\n\t"
        "s_mov_b32 exec_hi, 0x0fffffff\n\t"
        "buffer_load_dwordx4 %[vwo], %[srdw], s60 offen lds\n\t"
        "s_mov_b32 exec_hi, -1\n\t"
        "v_exp_f32 v120, v120\n\t"
        "v_exp_f32 v121, v121\n\t"
        "v_exp_f32 v122, v122\n\t"
        "v_exp_f32 v123, v123\n\t"
        "v_cvt_pk_bf16_f32 v152, v120, v121\n\t"
        "v_cvt_pk_bf16_f32 v153, v122, v123\n\t"
        "v_mfma_f32_32x32x16_bf16 v[32:47], v[184:187], v[156:159], v[32:47]\n\t"
        "ds_read_b128 v[180:183], %[va16] offset:19456\n\t"
        "v_exp_f32 v124, v124\n\t"
        "v_exp_f32 v125, v125\n\t"
        "v_exp_f32 v126, v126\n\t"
        "v_exp_f32 v127, v127\n\t"
        "v_cvt_pk_bf16_f32 v154, v124, v125\n\t"
        "v_cvt_pk_bf16_f32 v155, v126, v127\n\t"
        "s_waitcnt lgkmcnt(3)\n\t"
        "v_mfma_f32_32x32x8_bf16 v[136:151], v[178:179], v[230:231], v[136:151]\n\t"
        "ds_read_b64 v[164:165], %[va8] offset:22528\n\t"
        "s_add_u32 s60, %[swid], 72\n\t"
        "s_min_u32 s60, s60, 106\n\t"
        "s_mul_i32 s61, s60, 960\n\t"
        "s_add_u32 m0, s61, %[sdst]\n\t"
        "s_mul_i32 s60, s60, 7680\n\t"
        "s_add_u32 s60, s60, %[scol]\n\t"
        "s_mov_b32 exec_hi, 0x0fffffff\n\t"
        "buffer_load_dwordx4 %[vwo], %[srdw], s60 offen lds\n\t"
        "s_mov_b32 exec_hi, -1\n\t"
        "v_exp_f32 v128, v128\n\t"
        "v_exp_f32 v129, v129\n\t"
        "v_exp_f32 v130, v130\n\t"
        "v_exp_f32 v131, v131\n\t"
        "v_cvt_pk_bf16_f32 v156, v128, v129\n\t"
        "v_cvt_pk_bf16_f32 v157, v130, v131\n\t"
        "v_mfma_f32_32x32x16_bf16 v[32:47], v[166:169], v[152:155], v[32:47]\n\t"
        "ds_read_b128 v[184:187], %[va16] offset:20480\n\t"
        "v_exp_f32 v132, v132\n\t"
        "v_exp_f32 v133, v133\n\t"
        "v_exp_f32 v134, v134\n\t"
        "v_exp_f32 v135, v135\n\t"
        "v_cvt_pk_bf16_f32 v158, v132, v133\n\t"
        "v_cvt_pk_bf16_f32 v159, v134, v135\n\t"
        "s_waitcnt lgkmcnt(3)\n\t"
        "v_mfma_f32_32x32x16_bf16 v[120:135], v[160:163], v[226:229], 0\n\t"
        "ds_read_b128 v[174:177], %[va16] offset:25088\n\t"
        "s_add_u32 s60, %[swid], 80\n\t"
        "s_min_u32 s60, s60, 106\n\t"
        "s_mul_i32 s61, s60, 960\n\t"
        "s_add_u32 m0, s61, %[sdst]\n\t"
        "s_mul_i32 s60, s60, 7680\n\t"
        "s_add_u32 s60, s60, %[scol]\n\t"
        "s_mov_b32 exec_hi, 0x0fffffff\n\t"
        "buffer_load_dwordx4 %[vwo], %[srdw], s60 offen lds\n\t"
        "s_mov_b32 exec_hi, -1\n\t"
        "v_exp_f32 v136, v136\n\t"
        "v_exp_f32 v137, v137\n\t"
        "v_exp_f32 v138, v138\n\t"
        "v_exp_f32 v139, v139\n\t"
        "v_cvt_pk_bf16_f32 v152, v136, v137\n\t"
        "v_cvt_pk_bf16_f32 v153, v138, v139\n\t"
        "v_mfma_f32_32x32x16_bf16 v[32:47], v[170:173], v[156:159], v[32:47]\n\t"
        "ds_read_b128 v[166:169], %[va16] offset:23040\n\t"
        "v_exp_f32 v140, v140\n\t"
        "v_exp_f32 v141, v141\n\t"
        "v_exp_f32 v142, v142\n\t"
        "v_exp_f32 v143, v143\n\t"
        "v_cvt_pk_bf16_f32 v154, v140, v141\n\t"
        "v_cvt_pk_bf16_f32 v155, v142, v143\n\t"
        "s_waitcnt lgkmcnt(3)\n\t"
        "v_mfma_f32_32x32x8_bf16 v[120:135], v[164:165], v[230:231], v[120:135]\n\t"
        "ds_read_b64 v[178:179], %[va8] offset:26112\n\t"
        "s_add_u32 s60, %[swid], 88\n\t"
        "s_min_u32 s60, s60, 106\n\t"
        "s_mul_i32 s61, s60, 960\n\t"
        "s_add_u32 m0, s61, %[sdst]\n\t"
        "s_mul_i32 s60, s60, 7680\n\t"
        "s_add_u32 s60, s60, %[scol]\n\t"
        "s_mov_b32 exec_hi, 0x0fffffff\n\t"
        "buffer_load_dwordx4 %[vwo], %[srdw], s60 offen lds\n\t"
        "s_mov_b32 exec_hi, -1\n\t"
        "v_exp_f32 v144, v144\n\t"
        "v_exp_f32 v145, v145\n\t"
        "v_exp_f32 v146, v146\n\t"
        "v_exp_f32 v147, v147\n\t"
        "v_cvt_pk_bf16_f32 v156, v144, v145\n\t"
        "v_cvt_pk_bf16_f32 v157, v146, v147\n\t"
        "v_mfma_f32_32x32x16_bf16 v[32:47], v[180:183], v[152:155], v[32:47]\n\t"
        "ds_read_b128 v[170:173], %[va16] offset:24064\n\t"
        "v_exp_f32 v148, v148\n\t"
        "v_exp_f32 v149, v149\n\t"
        "v_exp_f32 v150, v150\n\t"
        "v_exp_f32 v151, v151\n\t"
        "v_cvt_pk_bf16_f32 v158, v148, v149\n\t"
        "v_cvt_pk_bf16_f32 v159, v150, v151\n\t"
        "s_waitcnt lgkmcnt(3)\n\t"
        "v_mfma_f32_32x32x16_bf16 v[136:151], v[174:177], v[226:229], 0\n\t"
        "ds_read_b128 v[160:163], %[va16] offset:28672\n\t"
        "s_add_u32 s60, %[swid], 96\n\t"
        "s_min_u32 s60, s60, 106\n\t"
        "s_mul_i32 s61, s60, 960\n\t"
        "s_add_u32 m0, s61, %[sdst]\n\t"
        "s_mul_i32 s60, s60, 7680\n\t"
        "s_add_u32 s60, s60, %[scol]\n\t"
        "s_mov_b32 exec_hi, 0x0fffffff\n\t"
        "buffer_load_dwordx4 %[vwo], %[srdw], s60 offen lds\n\t"
        "s_mov_b32 exec_hi, -1\n\t"
        "v_exp_f32 v120, v120\n\t"
        "v_exp_f32 v121, v121\n\t"
        "v_exp_f32 v122, v122\n\t"
        "v_exp_f32 v123, v123\n\t"
        "v_cvt_pk_bf16_f32 v152, v120, v121\n\t"
        "v_cvt_pk_bf16_f32 v153, v122, v123\n\t"
        "v_mfma_f32_32x32x16_bf16 v[32:47], v[184:187], v[156:159], v[32:47]\n\t"
        "ds_read_b128 v[180:183], %[va16] offset:26624\n\t"
        "v_exp_f32 v124, v124\n\t"
        "v_exp_f32 v125, v125\n\t"
        "v_exp_f32 v126, v126\n\t"
        "v_exp_f32 v127, v127\n\t"
        "v_cvt_pk_bf16_f32 v154, v124, v125\n\t"
        "v_cvt_pk_bf16_f32 v155, v126, v127\n\t"
        "s_waitcnt lgkmcnt(3)\n\t"
        "v_mfma_f32_32x32x8_bf16 v[136:151], v[178:179], v[230:231], v[136:151]\n\t"
        "ds_read_b64 v[164:165], %[va8] offset:29696\n\t"
        "s_add_u32 s60, %[swid], 104\n\t"
        "s_min_u32 s60, s60, 106\n\t"
        "s_mul_i32 s61, s60, 960\n\t"
        "s_add_u32 m0, s61, %[sdst]\n\t"
        "s_mul_i32 s60, s60, 7680\n\t"
        "s_add_u32 s60, s60, %[scol]\n\t"
        "s_mov_b32 exec_hi, 0x0fffffff\n\t"
        "buffer_load_dwordx4 %[vwo], %[srdw], s60 offen lds\n\t"
        "s_mov_b32 exec_hi, -1\n\t"
        "v_exp_f32 v128, v128\n\t"
        "v_exp_f32 v129, v129\n\t"
        "v_exp_f32 v130, v130\n\t"
        "v_exp_f32 v131, v131\n\t"
        "v_cvt_pk_bf16_f32 v156, v128, v129\n\t"
        "v_cvt_pk_bf16_f32 v157, v130, v131\n\t"
        "v_mfma_f32_32x32x16_bf16 v[32:47], v[166:169], v[152:155], v[32:47]\n\t"
        "ds_read_b128 v[184:187], %[va16] offset:27648\n\t"
        "v_exp_f32 v132, v132\n\t"
        "v_exp_f32 v133, v133\n\t"
        "v_exp_f32 v134, v134\n\t"
        "v_exp_f32 v135, v135\n\t"
        "v_cvt_pk_bf16_f32 v158, v132, v133\n\t"
        "v_cvt_pk_bf16_f32 v159, v134, v135\n\t"
        "s_waitcnt lgkmcnt(3)\n\t"
        "v_mfma_f32_32x32x16_bf16 v[120:135], v[160:163], v[226:229], v[194:209]\n\t"
        "v_exp_f32 v136, v136\n\t"
        "v_exp_f32 v137, v137\n\t"
        "v_exp_f32 v138, v138\n\t"
        "v_exp_f32 v139, v139\n\t"
        "v_cvt_pk_bf16_f32 v152, v136, v137\n\t"
        "v_cvt_pk_bf16_f32 v153, v138, v139\n\t"
        "v_mfma_f32_32x32x16_bf16 v[32:47], v[170:173], v[156:159], v[32:47]\n\t"
        "ds_read_b128 v[166:169], %[va16] offset:30208\n\t"
        "v_exp_f32 v140, v140\n\t"
        "v_exp_f32 v141, v141\n\t"
        "v_exp_f32 v142, v142\n\t"
        "v_exp_f32 v143, v143\n\t"
        "v_cvt_pk_bf16_f32 v154, v140, v141\n\t"
        "v_cvt_pk_bf16_f32 v155, v142, v143\n\t"
        "s_waitcnt lgkmcnt(2)\n\t"
        "v_mfma_f32_32x32x8_bf16 v[120:135], v[164:165], v[230:231], v[120:135]\n\t"
        "v_exp_f32 v144, v144\n\t"
        "v_exp_f32 v145, v145\n\t"
        "v_exp_f32 v146, v146\n\t"
        "v_exp_f32 v147, v147\n\t"
        "v_cvt_pk_bf16_f32 v156, v144, v145\n\t"
        "v_cvt_pk_bf16_f32 v157, v146, v147\n\t"
        "v_mfma_f32_32x32x16_bf16 v[32:47], v[180:183], v[152:155], v[32:47]\n\t"
        "ds_read_b128 v[170:173], %[va16] offset:31232\n\t"
        "v_exp_f32 v148, v148\n\t"
        "v_exp_f32 v149, v149\n\t"
        "v_exp_f32 v150, v150\n\t"
        "v_exp_f32 v151, v151\n\t"
        "v_cvt_pk_bf16_f32 v158, v148, v149\n\t"
        "v_cvt_pk_bf16_f32 v159, v150, v151\n\t"
        "v_exp_f32 v120, v120\n\t"
        "v_exp_f32 v121, v121\n\t"
        "v_exp_f32 v122, v122\n\t"
        "v_exp_f32 v123, v123\n\t"
        "v_cvt_pk_bf16_f32 v152, v120, v121\n\t"
        "v_cvt_pk_bf16_f32 v153, v122, v123\n\t"
        "s_waitcnt lgkmcnt(2)\n\t"
        "v_mfma_f32_32x32x16_bf16 v[32:47], v[184:187], v[156:159], v[32:47]\n\t"
        "v_exp_f32 v124, v124\n\t"
        "v_exp_f32 v125, v125\n\t"
        "v_exp_f32 v126, v126\n\t"
        "v_exp_f32 v127, v127\n\t"
        "v_cvt_pk_bf16_f32 v154, v124, v125\n\t"
        "v_cvt_pk_bf16_f32 v155, v126, v127\n\t"
        "v_exp_f32 v128, v128\n\t"
        "v_exp_f32 v129, v129\n\t"
        "v_exp_f32 v130, v130\n\t"
        "v_exp_f32 v131, v131\n\t"
        "v_cvt_pk_bf16_f32 v156, v128, v129\n\t"
        "v_cvt_pk_bf16_f32 v157, v130, v131\n\t"
        "s_waitcnt lgkmcnt(1)\n\t"
        "v_mfma_f32_32x32x16_bf16 v[32:47], v[166:169], v[152:155], v[32:47]\n\t"
        "v_exp_f32 v132, v132\n\t"
        "v_exp_f32 v133, v133\n\t"
        "v_exp_f32 v134, v134\n\t"
        "v_exp_f32 v135, v135\n\t"
        "v_cvt_pk_bf16_f32 v158, v132, v133\n\t"
        "v_cvt_pk_bf16_f32 v159, v134, v135\n\t"
        "s_waitcnt lgkmcnt(0)\n\t"
        "s_nop 1\n\t"
        "v_mfma_f32_32x32x16_bf16 v[32:47], v[170:173], v[156:159], v[32:47]\n\t"
        "s_nop 15\n\t"
            : "=&{v[32:47]}"(oa), "+{v[230:231]}"(qa8)
            : [va16] "v"(va16), [va8] "v"(va8), [vd16] "v"(vd16), [vd8] "v"(vd8), [vpub] "v"(vpub), [vwo] "v"(vwo), "{v[194:209]}"(cdiag), "{v[210:225]}"(cfar), "{v[226:229]}"(qa16),
              [qz] "s"(qz), [srdf] "s"(srdf), [srdw] "s"(srdw), [swid] "s"(wid), [sdst] "s"(ldsb + LDS_WO), [scol] "s"(pnt * 160), "{s[72:73]}"(tag)
            : "v48", "v49", "v50", "v51", "v52", "v53", "v54", "v55", "v120", "v121", "v122", "v123", "v124", "v125", "v126", "v127", "v128", "v129", "v130", "v131", "v132", "v133", "v134", "v135", "v136", "v137", "v138", "v139", "v140", "v141", "v142", "v143", "v144", "v145", "v146", "v147", "v148", "v149", "v150", "v151", "v152", "v153", "v154", "v155", "v156", "v157", "v158", "v159", "v160", "v161", "v162", "v163", "v164", "v165", "v166", "v167", "v168", "v169", "v170", "v171", "v172", "v173", "v174", "v175", "v176", "v177", "v178", "v179", "v180", "v181", "v182", "v183", "v184", "v185", "v186", "v187", "v189", "v190", "v191", "v192", "v193", "s60", "s61", "s62", "s63", "scc", "memory");
    }
    STAMP(6);
    {
        float o[12];
#pragma unroll
        for (int i = 0; i < 12; ++i) o[i] = oa[i];
        finish_block(o, qfA, kvA, qb < 8 ? 0 : qb - 8, qb + 1, 256, qb * 32, ro, (qb * 32 + head) * QT);
    }
    BARRIER_DRAINED();
    if (wid == 4) publish(ws, SY_TA + blockIdx.x * 64, tag);
    if (wid == 4) wait_flags<1>(ws, (pbr ? SY_TB : SY_TA) + ((pj * 4 + (l31 & 3)) * 8 + (l31 >> 2)) * 64, 0, tag);
    asm volatile("s_barrier" ::: "memory");
    STAMP(7);
    {
        const int l15 = lane & 15, kq = lane >> 4, kh = wid >> 2, rb = wid & 3;
        const unsigned of = (pbr ? QF_BYTES : 0u) + (pmt * 4 + rb) * 32 * QT;
        u32x4 of16[8][2]; u32x2 of8[2][2][2];
#pragma unroll
        for (int ks = 0; ks < 8; ++ks)
#pragma unroll
            for (int u = 0; u < 2; ++u) of16[ks][u] = ld16(ro, of + (16 * kh + 2 * ks + (kq >> 1)) * QT + ((kq & 1) * 32 + 16 * u + l15) * 16);
#pragma unroll
        for (int k8 = 0; k8 < 2; ++k8)
#pragma unroll
            for (int u = 0; u < 2; ++u)
#pragma unroll
                for (int t = 0; t < 2; ++t) of8[k8][u][t] = ld8(ro, of + (16 * kh + 8 * k8 + 4 * t + kq) * QT + 1024 + (16 * u + l15) * 8);
        asm volatile("" : "+v"(of16[0][0]), "+v"(of16[0][1]), "+v"(of16[1][0]), "+v"(of16[1][1]), "+v"(of16[2][0]), "+v"(of16[2][1]), "+v"(of16[3][0]), "+v"(of16[3][1]),
                          "+v"(of16[4][0]), "+v"(of16[4][1]), "+v"(of16[5][0]), "+v"(of16[5][1]), "+v"(of16[6][0]), "+v"(of16[6][1]), "+v"(of16[7][0]), "+v"(of16[7][1]),
                          "+v"(of8[0][0][0]), "+v"(of8[0][0][1]), "+v"(of8[0][1][0]), "+v"(of8[0][1][1]), "+v"(of8[1][0][0]), "+v"(of8[1][0][1]), "+v"(of8[1][1][0]), "+v"(of8[1][1][1]));
        STAMP(9);
        f32x4 acc[2][5];
#pragma unroll
        for (int u = 0; u < 2; ++u)
#pragma unroll
            for (int v = 0; v < 5; ++v) acc[u][v] = f32x4{};
        LDSP const char* wl = (LDSP const char*)smem + LDS_WO + (l15 >> 2) * 160 + (lane & 3) * 8 + kh * 320 * 160;
#pragma unroll
        for (int ks = 0; ks < 8; ++ks) {
            LDSP const char* w0 = wl + (20 * (2 * ks + (kq >> 1)) + 4 * (kq & 1)) * 160;
#pragma unroll
            for (int v = 0; v < 5; ++v) {
                const bf16x8 wf = cat(lds_tr(w0 + v * 32), lds_tr(w0 + 8 * 160 + v * 32));
#pragma unroll
                for (int u = 0; u < 2; ++u) acc[u][v] = MFMA16(wf, __builtin_bit_cast(bf16x8, of16[ks][u]), acc[u][v]);
            }
        }
#pragma unroll
        for (int k8 = 0; k8 < 2; ++k8) {
            LDSP const char* w0 = wl + (20 * (8 * k8 + kq) + 16) * 160;
#pragma unroll
            for (int v = 0; v < 5; ++v) {
                const bf16x8 wf = cat(lds_tr(w0 + v * 32), lds_tr(w0 + 80 * 160 + v * 32));
#pragma unroll
                for (int u = 0; u < 2; ++u) { const u32x4 ov = {of8[k8][u][0][0], of8[k8][u][0][1], of8[k8][u][1][0], of8[k8][u][1][1]};
                    acc[u][v] = MFMA16(wf, __builtin_bit_cast(bf16x8, ov), acc[u][v]); }
            }
        }
        STAMP(10);
        LDSP char* red = (LDSP char*)smem + LDS_AIMG + lane * 16;
#pragma unroll
        for (int v = 0; v < 5; ++v) *(LDSP f32x4*)(red + ((rb * 2 + kh) * 5 + v) * 1024) = kh == 0 ? acc[1][v] : acc[0][v];
        asm volatile("s_waitcnt lgkmcnt(0)\n\ts_barrier" ::: "memory");
        char* dst = out + (size_t)(pmt * 128 + rb * 32 + 16 * kh + l15) * (2 * DM * 2) + pbr * (DM * 2) + (pnt * 80 + 4 * kq) * 2;
#pragma unroll
        for (int v = 0; v < 5; ++v) { const f32x4 a = (kh == 0 ? acc[0][v] : acc[1][v]) + *(LDSP const f32x4*)(red + ((rb * 2 + (kh ^ 1)) * 5 + v) * 1024);
            const u32x2 o = {cvtpk(a[0], a[1]), cvtpk(a[2], a[3])}; *(u32x2*)(dst + v * 32) = o; }
    }
    STAMP(8);
    asm volatile("ktext_end_lbl:");
}

extern "C" void kernel_launch(void* const* d_in, const int* in_sizes, int n_in, void* d_out, int out_size, void* d_ws, size_t ws_size, hipStream_t stream) {
    (void)in_sizes; (void)n_in; (void)out_size; (void)ws_size;
    (void)hipFuncSetAttribute((const void*)mha2_fused, hipFuncAttributeMaxDynamicSharedMemorySize, LDS_BYTES);
    mha2_fused<<<256, 512, LDS_BYTES, stream>>>((const char*)d_in[0], (const char*)d_in[1], (const char*)d_in[2], (const char*)d_in[3], (const char*)d_in[4], (const char*)d_in[5],
                                                (char*)d_ws, (char*)d_out);
}
```
